```python
import torch

def _softmax(x, axis=-1):
    return torch.softmax(x, dim=axis)

def _gelu(x):
    return torch.nn.functional.gelu(x, approximate='tanh')

def _silu(x):
    return torch.nn.functional.silu(x)

def _sigmoid(x):
    return torch.sigmoid(x)

def _layernorm(x, axis=-1, eps=1e-05):
    m = x.mean(dim=axis, keepdim=True)
    v = x.var(dim=axis, keepdim=True, unbiased=False)
    return (x - m) / torch.sqrt(v + eps)

def _scatter_add(tgt, idx, src, axis=0):
    return tgt.clone().index_add_(axis, idx, src)

def _segment_sum(src, seg, n):
    out = torch.zeros((n,) + src.shape[1:], dtype=src.dtype)
    return out.index_add_(0, seg, src)

def _pad(x, pw):
    flat = tuple((v for pair in reversed(pw) for v in pair))
    return torch.nn.functional.pad(x, flat)
kernel_name = 'adaLN_cond__compressed_kv_cla__dense_activation_squared_relu__decode'

def setup_inputs():

    _w = lambda a: (lambda t: t.to(torch.bfloat16) if t.is_floating_point() else t)(torch.as_tensor(a))

    def _A_setup():
        B, H, Sq, Sk, D, M = (2, 32, 1, 4096, 128, 640)
        Dc = 4 * D
        return {'x': _w((lambda *s: torch.randn(*s, dtype=torch.bfloat16) * 0.05)(B, Sq, M)), 'c_kv': _w((lambda *s: torch.randn(*s, dtype=torch.bfloat16) * 1.6)(B, Sk, Dc)), 'w_q': _w((lambda *s: torch.randn(*s, dtype=torch.bfloat16) * 0.8)(M, H, D) * M ** (-0.5)), 'w_uk': _w((lambda *s: torch.randn(*s, dtype=torch.bfloat16) * 0.8)(Dc, H, D) * Dc ** (-0.5)), 'w_uv': _w((lambda *s: torch.randn(*s, dtype=torch.bfloat16) * 1.6)(Dc, H, D) * Dc ** (-0.5)), 'w_o': _w((lambda *s: torch.randn(*s, dtype=torch.bfloat16) * 1.6)(H, D, M) * (H * D) ** (-0.5))}
    _A_in = _A_setup()

    def _B_setup():
        B, S, D, F = (2, 1, 640, 1728)
        return {'x': _w((lambda *s: torch.randn(*s, dtype=torch.bfloat16) * 0.05)(B, S, D)), 'w1': _w((lambda *s: torch.randn(*s, dtype=torch.bfloat16) * 0.05)(D, F)), 'b1': _w((lambda *s: torch.randn(*s, dtype=torch.bfloat16) * 0.2)(F)), 'w2': _w((lambda *s: torch.randn(*s, dtype=torch.bfloat16) * 0.05)(F, D)), 'b2': _w((lambda *s: torch.randn(*s, dtype=torch.bfloat16) * 1.6)(D))}
    _B_in = _B_setup()
    return {'A_x': _A_in['x'], 'A_c_kv': _A_in['c_kv'], 'A_w_q': _A_in['w_q'], 'A_w_uk': _A_in['w_uk'], 'A_w_uv': _A_in['w_uv'], 'A_w_o': _A_in['w_o'], 'B_x': _B_in['x'], 'B_w1': _B_in['w1'], 'B_b1': _B_in['b1'], 'B_w2': _B_in['w2'], 'B_b2': _B_in['b2'], 'c': _w((lambda *s: torch.randn(*s, dtype=torch.bfloat16) * 0.1)(2, 640) * 8.0), 'w_mod': _w((lambda *s: torch.randn(*s, dtype=torch.bfloat16) * 0.1)(640, 6 * 640) * 640 ** (-0.5) * 8.0)}

def reference(A_x, A_c_kv, A_w_q, A_w_uk, A_w_uv, A_w_o, B_x, B_w1, B_b1, B_w2, B_b2, c, w_mod):

    def _A_ref(x, c_kv, w_q, w_uk, w_uv, w_o):
        B, Sq, M = (x.shape[0], x.shape[1], x.shape[2])
        Sk = c_kv.shape[1]
        H, D = (w_q.shape[1], w_q.shape[2])
        q = torch.einsum('bqm,mhd->bhqd', x, w_q)
        k = torch.einsum('bkc,chd->bhkd', c_kv, w_uk)
        v = torch.einsum('bkc,chd->bhkd', c_kv, w_uv)
        scores = torch.einsum('bhqd,bhkd->bhqk', q, k) * D ** (-0.5)
        off = Sk - Sq
        qi = torch.arange(Sq)[:, None]
        kj = torch.arange(Sk)[None, :]
        cmask = kj <= qi + off
        scores = torch.where(cmask[None, None], scores, -1000000000.0)
        p = _softmax(scores, axis=-1)
        ctx = torch.einsum('bhqk,bhkd->bhqd', p, v)
        return torch.einsum('bhqd,hdm->bqm', ctx, w_o)

    def _B_ref(x, w1, b1, w2, b2):
        h = torch.einsum('bsd,df->bsf', x, w1) + b1
        zero = torch.zeros_like(h)
        r = torch.where(h > zero, h, zero)
        h = r * r
        out = torch.einsum('bsf,fd->bsd', h, w2) + b2
        return out
    x = A_x
    mod = (_silu(c) @ w_mod).reshape(c.shape[0], 6, 1, -1)
    sa, ca, ga, sb, cb, gb = (mod[:, 0].clone(), mod[:, 1].clone(), mod[:, 2].clone(), mod[:, 3].clone(), mod[:, 4].clone(), mod[:, 5].clone())
    h = _layernorm(x, axis=-1) * (1 + ca) + sa
    x = x + ga * _A_ref(h, A_c_kv, A_w_q, A_w_uk, A_w_uv, A_w_o)
    h = _layernorm(x, axis=-1) * (1 + cb) + sb
    x = x + gb * _B_ref(h, B_w1, B_b1, B_w2, B_b2)
    return x
```

```cpp
#include <hip/hip_runtime.h>
#include <hip/hip_bf16.h>
#include <stdint.h>

typedef unsigned short u16;
typedef unsigned v4u __attribute__((ext_vector_type(4)));

__device__ __forceinline__ float bf2f(u16 b) { return __uint_as_float(((unsigned)b) << 16); }
__device__ __forceinline__ float blo(unsigned w) { return __uint_as_float(w << 16); }
__device__ __forceinline__ float bhi(unsigned w) { return __uint_as_float(w & 0xffff0000u); }
__device__ __forceinline__ u16 f2bf(float f) { __hip_bfloat16 h = __float2bfloat16(f); return *reinterpret_cast<u16*>(&h); }
__device__ __forceinline__ float rbf(float f) { return bf2f(f2bf(f)); }

constexpr int B = 2, H = 32, SK = 4096, D = 128, M = 640, DC = 512, F = 1728;

template <int K>
__device__ __forceinline__ void gemv64(const float* xs, const u16* W, int ldw, int n0, float* red, float& o0, float& o1) {
  const int t = threadIdx.x, cg = t & 7, kg = t >> 3;
  float a0[8], a1[8];
#pragma unroll
  for (int i = 0; i < 8; ++i) { a0[i] = 0.f; a1[i] = 0.f; }
#pragma unroll 4
  for (int k = kg; k < K; k += 32) {
    v4u w = *reinterpret_cast<const v4u*>(W + (size_t)k * ldw + n0 + cg * 8);
    float x0 = xs[k], x1 = xs[K + k];
#pragma unroll
    for (int i = 0; i < 4; ++i) {
      float lo = blo(w[i]), hi = bhi(w[i]);
      a0[2 * i] += x0 * lo; a0[2 * i + 1] += x0 * hi;
      a1[2 * i] += x1 * lo; a1[2 * i + 1] += x1 * hi;
    }
  }
#pragma unroll
  for (int i = 0; i < 8; ++i) { red[(kg * 2 + 0) * 64 + cg * 8 + i] = a0[i]; red[(kg * 2 + 1) * 64 + cg * 8 + i] = a1[i]; }
  __syncthreads();
  o0 = 0.f; o1 = 0.f;
  if (t < 64) {
    for (int g = 0; g < 32; ++g) { o0 += red[(g * 2 + 0) * 64 + t]; o1 += red[(g * 2 + 1) * 64 + t]; }
  }
}

__global__ void __launch_bounds__(256) k_mod(const u16* c, const u16* wmod, float* mod) {
  __shared__ float xs[2 * M];
  __shared__ float red[32 * 2 * 64];
  for (int i = threadIdx.x; i < 2 * M; i += 256) { float v = bf2f(c[i]); xs[i] = rbf(v / (1.f + __expf(-v))); }
  __syncthreads();
  float o0, o1; int n0 = blockIdx.x * 64;
  gemv64<M>(xs, wmod, 6 * M, n0, red, o0, o1);
  if (threadIdx.x < 64) { mod[n0 + threadIdx.x] = rbf(o0); mod[6 * M + n0 + threadIdx.x] = rbf(o1); }
}

__device__ __forceinline__ void ln_mod(const float* xin  , float* xs, const float* mod, int so, int co, float* red) {
  const int t = threadIdx.x, w = t >> 6, l = t & 63;
  if (w < 2) {
    float s = 0.f;
    for (int i = l; i < M; i += 64) s += xin[w * M + i];
    for (int o = 32; o; o >>= 1) s += __shfl_xor(s, o);
    float mean = s / M;
    float v = 0.f;
    for (int i = l; i < M; i += 64) { float d = xin[w * M + i] - mean; v += d * d; }
    for (int o = 32; o; o >>= 1) v += __shfl_xor(v, o);
    if (l == 0) { red[w * 2] = rbf(mean); red[w * 2 + 1] = rbf(sqrtf(rbf(rbf(v / M) + 1e-5f))); }
  }
  __syncthreads();
  for (int i = t; i < 2 * M; i += 256) {
    int b = i / M, m = i % M;
    float ln = rbf(rbf(xin[i] - red[b * 2]) / red[b * 2 + 1]);
    xs[i] = rbf(rbf(ln * rbf(1.f + mod[b * 6 * M + co + m])) + mod[b * 6 * M + so + m]);
  }
  __syncthreads();
}

__global__ void __launch_bounds__(256) k_q(const u16* x, const float* mod, const u16* wq, float* q) {
  __shared__ float xin[2 * M];
  __shared__ float xs[2 * M];
  __shared__ float red[32 * 2 * 64];
  for (int i = threadIdx.x; i < 2 * M; i += 256) xin[i] = bf2f(x[i]);
  __syncthreads();
  ln_mod(xin, xs, mod, 0, M, red);
  float o0, o1; int n0 = blockIdx.x * 64;
  gemv64<M>(xs, wq, H * D, n0, red, o0, o1);
  if (threadIdx.x < 64) { q[n0 + threadIdx.x] = rbf(o0); q[H * D + n0 + threadIdx.x] = rbf(o1); }
}

__global__ void __launch_bounds__(256) k_qlat(const float* q, const u16* wuk, float* qlat) {
  const int h = blockIdx.x >> 3, cc = blockIdx.x & 7, t = threadIdx.x, dl = t & 15, r = t >> 4;
  float q0[8], q1[8];
#pragma unroll
  for (int i = 0; i < 8; ++i) { q0[i] = q[h * D + dl * 8 + i]; q1[i] = q[H * D + h * D + dl * 8 + i]; }
#pragma unroll
  for (int p = 0; p < 4; ++p) {
    int c = cc * 64 + p * 16 + r;
    v4u w = *reinterpret_cast<const v4u*>(wuk + ((size_t)c * H + h) * D + dl * 8);
    float s0 = 0.f, s1 = 0.f;
#pragma unroll
    for (int i = 0; i < 4; ++i) {
      float lo = blo(w[i]), hi = bhi(w[i]);
      s0 += q0[2 * i] * lo + q0[2 * i + 1] * hi; s1 += q1[2 * i] * lo + q1[2 * i + 1] * hi;
    }
    for (int o = 8; o; o >>= 1) { s0 += __shfl_xor(s0, o); s1 += __shfl_xor(s1, o); }
    if (dl == 0) { qlat[(0 * H + h) * DC + c] = s0; qlat[(1 * H + h) * DC + c] = s1; }
  }
}

constexpr int NS = 32, KC = SK / NS;
__global__ void __launch_bounds__(256) k_attn(const float* qlat, const u16* ckv, float* pm, float* pl, float* po) {
  __shared__ float qs[8][DC];
  __shared__ float sc[8][KC];
  __shared__ float ml[8][2];
  const int bid = blockIdx.x, s = bid % NS, hg = (bid / NS) % 4, b = bid / (NS * 4), t = threadIdx.x, w = t >> 6, l = t & 63;
  for (int i = t; i < 8 * DC; i += 256) qs[i / DC][i % DC] = qlat[(b * H + hg * 8) * DC + i];
  __syncthreads();
  const u16* base = ckv + ((size_t)b * SK + s * KC) * DC;
  for (int k = w; k < KC; k += 4) {
    v4u cv = *reinterpret_cast<const v4u*>(base + (size_t)k * DC + l * 8);
    float cf[8];
#pragma unroll
    for (int i = 0; i < 4; ++i) { cf[2 * i] = blo(cv[i]); cf[2 * i + 1] = bhi(cv[i]); }
#pragma unroll
    for (int hh = 0; hh < 8; ++hh) {
      float a = 0.f;
#pragma unroll
      for (int i = 0; i < 8; ++i) a += cf[i] * qs[hh][l * 8 + i];
      for (int o = 32; o; o >>= 1) a += __shfl_xor(a, o);
      if (l == 0) sc[hh][k] = a * 0.08838834764831845f;
    }
  }
  __syncthreads();
  for (int hh = w * 2; hh < w * 2 + 2; ++hh) {
    float a = sc[hh][l], c2 = sc[hh][l + 64];
    float m = fmaxf(a, c2);
    for (int o = 32; o; o >>= 1) m = fmaxf(m, __shfl_xor(m, o));
    float e0 = __expf(a - m), e1 = __expf(c2 - m);
    sc[hh][l] = e0; sc[hh][l + 64] = e1;
    float su = e0 + e1;
    for (int o = 32; o; o >>= 1) su += __shfl_xor(su, o);
    if (l == 0) { ml[hh][0] = m; ml[hh][1] = su; }
  }
  __syncthreads();
  float acc[8][2];
#pragma unroll
  for (int hh = 0; hh < 8; ++hh) { acc[hh][0] = 0.f; acc[hh][1] = 0.f; }
  for (int k = 0; k < KC; ++k) {
    unsigned cv = *reinterpret_cast<const unsigned*>(base + (size_t)k * DC + t * 2);
    float lo = blo(cv), hi = bhi(cv);
#pragma unroll
    for (int hh = 0; hh < 8; ++hh) { float p = sc[hh][k]; acc[hh][0] += p * lo; acc[hh][1] += p * hi; }
  }
#pragma unroll
  for (int hh = 0; hh < 8; ++hh) {
    int row = (b * H + hg * 8 + hh) * NS + s;
    po[(size_t)row * DC + t * 2] = acc[hh][0]; po[(size_t)row * DC + t * 2 + 1] = acc[hh][1];
    if (t == 0) { pm[row] = ml[hh][0]; pl[row] = ml[hh][1]; }
  }
}

__global__ void __launch_bounds__(256) k_ctx(const float* pm, const float* pl, const float* po, const u16* wuv, float* ctx) {
  __shared__ float xs[2 * DC];
  __shared__ float red[32 * 2 * 64];
  __shared__ float wgt[2][NS];
  const int h = blockIdx.x >> 1, dh = blockIdx.x & 1, t = threadIdx.x;
  if (t < 64) {
    int b = t >> 5, s = t & 31; int row = (b * H + h) * NS + s;
    float m = pm[row], l = pl[row];
    float mx = m;
    for (int o = 16; o; o >>= 1) mx = fmaxf(mx, __shfl_xor(mx, o));
    float e = __expf(m - mx); float L = e * l;
    for (int o = 16; o; o >>= 1) L += __shfl_xor(L, o);
    wgt[b][s] = e / L;
  }
  __syncthreads();
  for (int i = t; i < 2 * DC; i += 256) {
    int b = i / DC, c = i % DC; float a = 0.f;
    for (int s = 0; s < NS; ++s) a += wgt[b][s] * po[(size_t)((b * H + h) * NS + s) * DC + c];
    xs[i] = a;
  }
  __syncthreads();
  float o0, o1; int n0 = h * D + dh * 64;
  gemv64<DC>(xs, wuv, H * D, n0, red, o0, o1);
  if (t < 64) { ctx[n0 + t] = rbf(o0); ctx[H * D + n0 + t] = rbf(o1); }
}

__global__ void __launch_bounds__(256) k_wo(const float* ctx, const u16* wo, float* part) {
  __shared__ float xs[2 * 256];
  __shared__ float red[32 * 2 * 64];
  const int ms = blockIdx.x % 10, ks = blockIdx.x / 10, t = threadIdx.x;
  for (int i = t; i < 512; i += 256) xs[i] = ctx[(i >> 8) * H * D + ks * 256 + (i & 255)];
  __syncthreads();
  float o0, o1;
  gemv64<256>(xs, wo + (size_t)ks * 256 * M, M, ms * 64, red, o0, o1);
  if (t < 64) { part[(ks * 2 + 0) * M + ms * 64 + t] = o0; part[(ks * 2 + 1) * M + ms * 64 + t] = o1; }
}

__global__ void __launch_bounds__(256) k_w1(const u16* x, const float* part, const float* mod, const u16* w1, const u16* b1, float* x1out, float* h1) {
  __shared__ float xin[2 * M];
  __shared__ float xs[2 * M];
  __shared__ float red[32 * 2 * 64];
  for (int i = threadIdx.x; i < 2 * M; i += 256) {
    int b = i / M, m = i % M; float a = 0.f;
    for (int ks = 0; ks < 16; ++ks) a += part[(ks * 2 + b) * M + m];
    float v = rbf(bf2f(x[i]) + rbf(mod[b * 6 * M + 2 * M + m] * rbf(a)));
    xin[i] = v;
    if (blockIdx.x == 0) x1out[i] = v;
  }
  __syncthreads();
  ln_mod(xin, xs, mod, 3 * M, 4 * M, red);
  float o0, o1; int n0 = blockIdx.x * 64;
  gemv64<M>(xs, w1, F, n0, red, o0, o1);
  if (threadIdx.x < 64) {
    float bb = bf2f(b1[n0 + threadIdx.x]);
    float r0 = fmaxf(rbf(rbf(o0) + bb), 0.f), r1 = fmaxf(rbf(rbf(o1) + bb), 0.f);
    h1[n0 + threadIdx.x] = rbf(r0 * r0); h1[F + n0 + threadIdx.x] = rbf(r1 * r1);
  }
}

__global__ void __launch_bounds__(256) k_w2(const float* h1, const float* x1, const float* mod, const u16* w2, const u16* b2, u16* out) {
  __shared__ float xs[2 * F];
  __shared__ float red[32 * 2 * 64];
  for (int i = threadIdx.x; i < 2 * F; i += 256) xs[i] = h1[i];
  __syncthreads();
  float o0, o1; int n0 = blockIdx.x * 64;
  gemv64<F>(xs, w2, M, n0, red, o0, o1);
  if (threadIdx.x < 64) {
    int m = n0 + threadIdx.x; float bb = bf2f(b2[m]);
    float y0 = rbf(rbf(o0) + bb), y1 = rbf(rbf(o1) + bb);
    out[m] = f2bf(x1[m] + rbf(mod[5 * M + m] * y0));
    out[M + m] = f2bf(x1[M + m] + rbf(mod[6 * M + 5 * M + m] * y1));
  }
}

extern "C" void kernel_launch(void* const* d_in, const int* in_sizes, int n_in,
                              void* d_out, int out_size, void* d_ws, size_t ws_size,
                              hipStream_t stream) {
  const u16* x = (const u16*)d_in[0]; const u16* ckv = (const u16*)d_in[1]; const u16* wq = (const u16*)d_in[2];
  const u16* wuk = (const u16*)d_in[3]; const u16* wuv = (const u16*)d_in[4]; const u16* wo = (const u16*)d_in[5];
  const u16* w1 = (const u16*)d_in[7]; const u16* b1 = (const u16*)d_in[8]; const u16* w2 = (const u16*)d_in[9];
  const u16* b2 = (const u16*)d_in[10]; const u16* c = (const u16*)d_in[11]; const u16* wmod = (const u16*)d_in[12];
  float* ws = (float*)d_ws;
  float* mod = ws; float* q = mod + 2 * 6 * M; float* qlat = q + 2 * H * D; float* pm = qlat + 2 * H * DC; float* pl = pm + 2 * H * NS;
  float* po = pl + 2 * H * NS; float* ctx = po + (size_t)2 * H * NS * DC; float* part = ctx + 2 * H * D; float* x1 = part + 16 * 2 * M; float* h1 = x1 + 2 * M;
  k_mod<<<60, 256, 0, stream>>>(c, wmod, mod);
  k_q<<<64, 256, 0, stream>>>(x, mod, wq, q);
  k_qlat<<<256, 256, 0, stream>>>(q, wuk, qlat);
  k_attn<<<256, 256, 0, stream>>>(qlat, ckv, pm, pl, po);
  k_ctx<<<64, 256, 0, stream>>>(pm, pl, po, wuv, ctx);
  k_wo<<<160, 256, 0, stream>>>(ctx, wo, part);
  k_w1<<<27, 256, 0, stream>>>(x, part, mod, w1, b1, x1, h1);
  k_w2<<<10, 256, 0, stream>>>(h1, x1, mod, w2, b2, (u16*)d_out);
}
```

```cpp
#include <hip/hip_runtime.h>
#include <hip/hip_bf16.h>
#include <stdint.h>

#define LAS __attribute__((address_space(3)))
#define GAS __attribute__((address_space(1)))
typedef unsigned short u16;
typedef unsigned v4u __attribute__((ext_vector_type(4)));
typedef unsigned v2u __attribute__((ext_vector_type(2)));
typedef float f4 __attribute__((ext_vector_type(4)));
typedef float f2 __attribute__((ext_vector_type(2)));
typedef short s8v __attribute__((ext_vector_type(8)));
typedef short s4v __attribute__((ext_vector_type(4)));
typedef __bf16 bf2v __attribute__((ext_vector_type(2)));
typedef __amdgpu_buffer_rsrc_t rsrc_t;

constexpr int SK = 4096, NM = 640, DC = 512, FF = 1728, HD = 4096;

constexpr int WS_E = 0;
constexpr int WS_H1 = 16384;
constexpr int WS_H2 = 81920;
constexpr int WS_H3 = 344064;
constexpr int WS_ST = 409600;
constexpr int WS_H5 = 425984;
constexpr int WS_H6 = 491520;
constexpr int WS_H7 = 524288;
constexpr int WS_MV = 532480;
constexpr int WS_CLR = WS_H3;
constexpr int WS_H4 = 2097152;
constexpr int WS_BYTES = 16 << 20;
constexpr int STAMP_OFF = 8 << 20;
constexpr unsigned SENT = 0xFFFFFFFFu;
constexpr unsigned SPIN_CAP = 30000u;

constexpr int L_CKV = 0, L_QS = 131072, L_AM = 147456;
constexpr int L_MX = L_AM, L_LS = L_AM + 256, L_PB = L_AM + 512, L_ARED = L_AM + 4608;
constexpr int L_WQ = 0, L_WUV = 40960, L_ROLE = 73728, L_CM = 155648;
constexpr int L_HS = L_CM, L_QH = L_CM + 640, L_SC = L_CM + 1152 + 128, L_CRED = L_CM + 3840;
constexpr int LDS_BYTES = 163840;

#define STAMP(i) do {} while (0)
#define STAMPW(i, w) do {} while (0)

struct Params {
  const u16 *x, *ckv, *wq, *wuk, *wuv, *wo, *w1, *b1, *w2, *b2, *c, *wmod;
  u16* out;
  unsigned char* ws;
};

__device__ __forceinline__ float blo(unsigned w) { return __uint_as_float(w << 16); }
__device__ __forceinline__ float bhi(unsigned w) { return __uint_as_float(w & 0xffff0000u); }
__device__ __forceinline__ unsigned pk(float lo, float hi) { f2 f = {lo, hi}; return __builtin_bit_cast(unsigned, __builtin_convertvector(f, bf2v)); }
__device__ __forceinline__ float rbf(float f) { return blo(pk(f, 0.f)); }
__device__ __forceinline__ unsigned nosent(unsigned v) { return v < 0xFFFFFFFEu ? v : 0xFFFFFFFEu; }
__device__ __forceinline__ f4 mfma(v4u a, v4u b, f4 c) { return __builtin_amdgcn_mfma_f32_16x16x32_bf16(__builtin_bit_cast(s8v, a), __builtin_bit_cast(s8v, b), c, 0, 0, 0); }
template <int C> __device__ __forceinline__ float dpp(float x) { return __builtin_bit_cast(float, __builtin_amdgcn_mov_dpp(__builtin_bit_cast(int, x), C, 0xf, 0xf, true)); }
__device__ __forceinline__ float sum16(float a, float b) { auto s = __builtin_amdgcn_permlane16_swap(__float_as_uint(a), __float_as_uint(b), false, false); return __uint_as_float(s[0]) + __uint_as_float(s[1]); }
__device__ __forceinline__ float sum32(float a, float b) { auto s = __builtin_amdgcn_permlane32_swap(__float_as_uint(a), __float_as_uint(b), false, false); return __uint_as_float(s[0]) + __uint_as_float(s[1]); }
__device__ __forceinline__ float max16(float a) { auto s = __builtin_amdgcn_permlane16_swap(__float_as_uint(a), __float_as_uint(a), false, false); return fmaxf(__uint_as_float(s[0]), __uint_as_float(s[1])); }
__device__ __forceinline__ float max32(float a) { auto s = __builtin_amdgcn_permlane32_swap(__float_as_uint(a), __float_as_uint(a), false, false); return fmaxf(__uint_as_float(s[0]), __uint_as_float(s[1])); }
__device__ __forceinline__ float rsum32(float v) {
  v += dpp<0xB1>(v); v += dpp<0x4E>(v); v += dpp<0x141>(v); v += dpp<0x140>(v); return sum16(v, v);
}
__device__ __forceinline__ float rdl(float v, int l) { return __builtin_bit_cast(float, __builtin_amdgcn_readlane(__builtin_bit_cast(int, v), l)); }
__device__ __forceinline__ float dot2(unsigned a, unsigned b, float c) { return __builtin_amdgcn_fdot2_f32_bf16(__builtin_bit_cast(bf2v, a), __builtin_bit_cast(bf2v, b), c, false); }

__device__ __forceinline__ v4u ld16(rsrc_t r, int off) { return __builtin_amdgcn_raw_buffer_load_b128(r, off, 0, 16); }
__device__ __forceinline__ v2u ld8(rsrc_t r, int off) { return __builtin_amdgcn_raw_buffer_load_b64(r, off, 0, 16); }
__device__ __forceinline__ unsigned ld4(rsrc_t r, int off) { return __builtin_amdgcn_raw_buffer_load_b32(r, off, 0, 16); }
__device__ __forceinline__ void st16(rsrc_t r, int off, v4u v) { __builtin_amdgcn_raw_buffer_store_b128(v, r, off, 0, 16); }
__device__ __forceinline__ void st8(rsrc_t r, int off, v2u v) { __builtin_amdgcn_raw_buffer_store_b64(v, r, off, 0, 16); }
__device__ __forceinline__ void st4(rsrc_t r, int off, unsigned v) { __builtin_amdgcn_raw_buffer_store_b32(v, r, off, 0, 16); }
__device__ __forceinline__ v4u gld16(const void* p) { return *(const GAS v4u*)p; }
__device__ __forceinline__ v4u gld16nt(const void* p) { return __builtin_nontemporal_load((const GAS v4u*)p); }
__device__ __forceinline__ void dma16(const void* gsrc, unsigned lds_dst) {
  unsigned keep;
  asm volatile("s_mov_b32 %0, m0\n\ts_mov_b32 m0, %2\n\ts_nop 0\n\tglobal_load_lds_dwordx4 %1, off nt\n\ts_mov_b32 m0, %0" : "=&s"(keep) : "v"(gsrc), "s"(lds_dst) : "memory");
}
#define BAR() asm volatile("s_waitcnt lgkmcnt(0)\n\ts_barrier" ::: "memory")
#define VMW(n) asm volatile("s_waitcnt vmcnt(" #n ")" ::: "memory")
#define RELOAD() asm volatile("" ::: "memory")
#define PIN4(v) asm volatile("" : "+v"(v))
__device__ __forceinline__ bool nos4(v4u v) { return (v.x != SENT) & (v.y != SENT) & (v.z != SENT) & (v.w != SENT); }
__device__ __forceinline__ int fsw(int k) { return (k & 3) ^ ((k & 4) ? 12 : 0) ^ ((k & 8) ? 4 : 0); }

#define LDS_RD128(off) (*(const LAS v4u*)(lds + (off)))
#define LDS_RD64(off) (*(const LAS v2u*)(lds + (off)))
#define LDS_RDF(off) (*(const LAS float*)(lds + (off)))
#define LDS_WR128(off, v) (*(LAS v4u*)(lds + (off)) = (v))
#define LDS_WR64(off, v) (*(LAS v2u*)(lds + (off)) = (v))
#define LDS_WR32(off, v) (*(LAS unsigned*)(lds + (off)) = (v))
#define LDS_WRF(off, v) (*(LAS float*)(lds + (off)) = (v))

template <int RB> __device__ __forceinline__ int swz(int row) {
  if (RB == 256) return 2 * ((row & 3) | (((row >> 3) & 1) << 2));
  if (RB == 128) return 2 * (((row >> 1) & 1) | (((row >> 3) & 1) << 1));
  if (RB == 64) return 2 * ((row >> 3) & 1);
  return 0;
}
__device__ __forceinline__ v2u trrd(LAS unsigned char* lds, int off) { return __builtin_bit_cast(v2u, __builtin_amdgcn_ds_read_tr16_b64_v4i16((LAS s4v*)(lds + off))); }
template <int RB> __device__ __forceinline__ v4u trfrag(LAS unsigned char* lds, int base, int row0, int ctile, int lane) {
  const int g4 = lane >> 4, q = (lane & 15) >> 2, p = lane & 3;
  const int r0 = row0 + 8 * g4 + q, r1 = r0 + 4, ch = 2 * ctile + (p >> 1);
  v2u a, b;
  if (RB == 32) {
    const int ra = r0 ^ (((r0 >> 3) & 1) << 2), rb = r1 ^ (((r1 >> 3) & 1) << 2);
    a = trrd(lds, base + ra * 32 + p * 8); b = trrd(lds, base + rb * 32 + p * 8);
  } else {
    a = trrd(lds, base + r0 * RB + ((ch ^ swz<RB>(r0)) << 4) + (p & 1) * 8);
    b = trrd(lds, base + r1 * RB + ((ch ^ swz<RB>(r1)) << 4) + (p & 1) * 8);
  }
  return (v4u){a.x, a.y, b.x, b.y};
}
template <int RB> __device__ __forceinline__ void dma_tile(const unsigned char* src, size_t ld, int base, int ins, int lane) {
  constexpr int CPR = RB / 16, RPI = 64 / CPR;
  const int rp = ins * RPI + lane / CPR, pos = lane % CPR;
  int row = rp, ch = pos ^ swz<RB>(rp);
  if (RB == 32) { row = rp ^ (((rp >> 3) & 1) << 2); ch = pos; }
  dma16(src + (size_t)row * ld + ch * 16, (unsigned)(base + ins * 1024));
}
__device__ __forceinline__ v4u silu8(v4u c) {
  v4u o;
#pragma unroll
  for (int i = 0; i < 4; ++i) { float a = blo(c[i]), b = bhi(c[i]); o[i] = pk(a / (1.f + __expf(-a)), b / (1.f + __expf(-b))); }
  return o;
}

__device__ __forceinline__ void a_path(const Params& p, LAS unsigned char* lds, rsrc_t ws, int g, int w, int lane
) {
  const int t = threadIdx.x, xcd = g & 7, r = g >> 3, b = xcd >> 2, s = (xcd & 3) * 8 + (r >> 1), hg = r & 1;
  const int i = lane & 15, g4 = lane >> 4;
  const bool s0 = g < 100;
  const int cb = g % 20, ks0 = g / 20;
  const unsigned char* ck = (const unsigned char*)p.ckv + ((size_t)b * SK + s * 128) * 1024;
  unsigned tag = 0;
  v4u sa[4];
  if (w < 4) tag = ld4(ws, WS_E) + 1u;
  if (s0) {
    if (w < 4) {
#pragma unroll
      for (int ks = 0; ks < 4; ++ks) sa[ks] = gld16(p.c + (i & 1) * NM + 128 * ks0 + 32 * ks + 8 * g4);
    }
    const unsigned char* src = (const unsigned char*)p.wmod + ((size_t)128 * ks0 * 3840 + 64 * cb) * 2;
    dma_tile<128>(src, 7680, L_QS, 2 * w, lane); dma_tile<128>(src, 7680, L_QS, 2 * w + 1, lane);
  }
  STAMP(1);
  if (w < 4) {
    if (s0) {
#pragma unroll
      for (int ks = 0; ks < 4; ++ks) { sa[ks] = silu8(sa[ks]); PIN4(sa[ks]); }
    }
    VMW(0);
  } else if (w < 7) {
#pragma unroll
    for (int n = 0; n < 12; ++n) { const int k = (w - 4) * 12 + n; dma16(ck + (size_t)k * 1024 + ((lane ^ fsw(k & 15)) << 4), (unsigned)(L_CKV + k * 1024)); }
    VMW(12);
  } else {
    if (s0) { const v4u sv = {SENT, SENT, SENT, SENT}; st16(ws, WS_CLR + g * 2048 + lane * 16, sv); st16(ws, WS_CLR + g * 2048 + 1024 + lane * 16, sv); }
    VMW(0);
  }
  BAR();
  STAMP(2);
  if (w < 4) {
    if (s0) {
      f4 acc = {0.f, 0.f, 0.f, 0.f};
      v4u bf[4];
#pragma unroll
      for (int ks = 0; ks < 4; ++ks) bf[ks] = trfrag<128>(lds, L_QS, 32 * ks, w, lane);
#pragma unroll
      for (int ks = 0; ks < 4; ++ks) acc = mfma(sa[ks], bf[ks], acc);
      if (lane < 16) st8(ws, WS_H1 + (ks0 * 1280 + 64 * cb + 16 * w + lane) * 8, (v2u){pk(acc[0], acc[1]), tag});
    }
    STAMP(3);
#pragma unroll
    for (int n = 0; n < 6; ++n) { const int k = 104 + w * 6 + n; dma16(ck + (size_t)k * 1024 + ((lane ^ fsw(k & 15)) << 4), (unsigned)(L_CKV + k * 1024)); }
    if (w < 2) {
      const int pr = min(w * 64 + lane, 99);
      const int off = WS_H1 + ((pr / 20) * 1280 + 64 * (pr % 20)) * 8;
      for (unsigned sp = 0; sp < SPIN_CAP; ++sp) { RELOAD(); const v2u v = ld8(ws, off); if (__all(v.y == tag)) break; }
    }
  } else {
#pragma unroll
    for (int n = 0; n < 17; ++n) { const int k = 36 + (w - 4) * 17 + n; dma16(ck + (size_t)k * 1024 + ((lane ^ fsw(k & 15)) << 4), (unsigned)(L_CKV + k * 1024)); }
  }
  VMW(0);
  BAR();
  STAMP(4);
  v4u fr[32];
  if (w < 4) {
#pragma unroll
    for (int T = 0; T < 2; ++T)
#pragma unroll
      for (int ks = 0; ks < 16; ++ks) fr[T * 16 + ks] = LDS_RD128(L_CKV + (32 * w + 16 * T + i) * 1024 + (((4 * ks + g4) ^ fsw(i)) << 4));
  } else {
    const int pw = w - 4;
#pragma unroll
    for (int u = 0; u < 4; ++u) {
      v4u X[8];
#pragma unroll
      for (int jj = 0; jj < 8; ++jj) { const int kl = 4 * g4 + (jj & 3); X[jj] = LDS_RD128(L_CKV + (32 * u + 16 * (jj >> 2) + kl) * 1024 + (((16 * pw + i) ^ fsw(kl)) << 4)); }
#pragma unroll
      for (int tt = 0; tt < 8; ++tt)
#pragma unroll
        for (int m = 0; m < 4; ++m) fr[u * 8 + tt][m] = __builtin_amdgcn_perm(X[2 * m + 1][tt >> 1], X[2 * m][tt >> 1], (tt & 1) ? 0x07060302u : 0x05040100u);
    }
  }
#pragma unroll
  for (int n = 0; n < 32; ++n) PIN4(fr[n]);
  STAMP(5);
  {
    const int hh = t >> 6, ch = t & 63;
    const int o0 = WS_H3 + (hg * 2) * 16384 + b * 8192 + hh * 1024 + ch * 16;
    v4u q0, q1;
    for (unsigned sp = 0; sp < SPIN_CAP; ++sp) { RELOAD(); q0 = ld16(ws, o0); q1 = ld16(ws, o0 + 16384); if (__all(nos4(q0) & nos4(q1))) break; }
    LDS_WR128(L_QS + hh * 1024 + ((ch ^ fsw(hh)) << 4), q0);
    LDS_WR128(L_QS + (hh + 8) * 1024 + ((ch ^ fsw(hh + 8)) << 4), q1);
  }
  BAR();
  STAMP(6);
  const bool r5 = r < 10, r7 = (r >= 10) & (r < 15);
  const int i5 = xcd * 5 + (r >> 1), ks5 = hg * 4 + i5 / 10, ms = i5 % 10, mt7 = xcd * 5 + (r - 10);
  const unsigned char* src5 = (const unsigned char*)p.wo + ((size_t)512 * ks5 * NM + 64 * ms) * 2;
  const unsigned char* src7 = (const unsigned char*)p.w2 + (size_t)16 * mt7 * 2;
  f4 sacc[2];
  if (w < 4) {
    sacc[0] = (f4){0.f, 0.f, 0.f, 0.f}; sacc[1] = sacc[0];
#pragma unroll
    for (int ks = 0; ks < 16; ++ks) {
      const v4u qf = LDS_RD128(L_QS + i * 1024 + (((4 * ks + g4) ^ fsw(i)) << 4));
      sacc[0] = mfma(fr[ks], qf, sacc[0]); sacc[1] = mfma(fr[16 + ks], qf, sacc[1]);
    }
    float mx = fmaxf(fmaxf(fmaxf(sacc[0][0], sacc[0][1]), fmaxf(sacc[0][2], sacc[0][3])), fmaxf(fmaxf(sacc[1][0], sacc[1][1]), fmaxf(sacc[1][2], sacc[1][3])));
    mx = max32(max16(mx));
    if (lane < 16) LDS_WRF(L_MX + lane * 16 + w * 4, mx);
  } else {
    if (r5) {
#pragma unroll
      for (int n = 0; n < 8; ++n) dma_tile<128>(src5, NM * 2, L_CKV, (w - 4) * 8 + n, lane);
    } else if (r7) {
#pragma unroll
      for (int n = 0; n < 2; ++n) dma_tile<32>(src7, NM * 2, L_CKV, (w - 4) * 2 + n, lane);
    }
  }
  BAR();
  STAMP(7);
  float mrow = 0.f;
  if (w < 4) {
    const f4 m4 = __builtin_bit_cast(f4, LDS_RD128(L_MX + i * 16));
    mrow = fmaxf(fmaxf(m4[0], m4[1]), fmaxf(m4[2], m4[3]));
    float pp[8]; float ls = 0.f;
#pragma unroll
    for (int n = 0; n < 8; ++n) { pp[n] = __builtin_amdgcn_exp2f(sacc[n >> 2][n & 3] - mrow); ls += pp[n]; }
    LDS_WR128(L_PB + w * 1024 + lane * 16, ((v4u){pk(pp[0], pp[1]), pk(pp[2], pp[3]), pk(pp[4], pp[5]), pk(pp[6], pp[7])}));
    ls = sum16(ls, ls); ls = sum32(ls, ls);
    if (lane < 16) LDS_WRF(L_LS + lane * 16 + w * 4, ls);
  }
  BAR();
  STAMP(8);
  if (w < 4) {
    if (w == 0 && lane < 16) {
      const f4 l4 = __builtin_bit_cast(f4, LDS_RD128(L_LS + lane * 16));
      st8(ws, WS_ST + b * 8192 + (hg * 32 + s) * 128 + lane * 8, (v2u){nosent(__float_as_uint(mrow)), nosent(__float_as_uint((l4[0] + l4[1]) + (l4[2] + l4[3])))});
    }
  } else {
    v4u pf[4];
#pragma unroll
    for (int u = 0; u < 4; ++u) pf[u] = LDS_RD128(L_PB + u * 1024 + lane * 16);
    f4 o[8];
#pragma unroll
    for (int tt = 0; tt < 8; ++tt) o[tt] = (f4){0.f, 0.f, 0.f, 0.f};
#pragma unroll
    for (int u = 0; u < 4; ++u)
#pragma unroll
      for (int tt = 0; tt < 8; ++tt) o[tt] = mfma(pf[u], fr[u * 8 + tt], o[tt]);
    const int ob = WS_H4 + ((hg * 16 + 4 * g4) * 4 + (w - 4)) * 16384 + (b ^ 1) * 8192 + s * 256 + i * 16;
#pragma unroll
    for (int rg = 0; rg < 4; ++rg)
      st16(ws, ob + rg * 65536, (v4u){nosent(pk(o[0][rg], o[1][rg])), nosent(pk(o[2][rg], o[3][rg])), nosent(pk(o[4][rg], o[5][rg])), nosent(pk(o[6][rg], o[7][rg]))});
    STAMPW(9, 4);
  }
  BAR();
  if (r5) {
#pragma unroll
    for (int n = 0; n < 4; ++n) dma_tile<128>(src5, NM * 2, L_CKV, 32 + w * 4 + n, lane);
    VMW(0);
    BAR();
    STAMP(10);
    v4u wf[8];
#pragma unroll
    for (int ks = 0; ks < 2; ++ks)
#pragma unroll
      for (int mt = 0; mt < 4; ++mt) { wf[ks * 4 + mt] = trfrag<128>(lds, L_CKV, 64 * w + 32 * ks, mt, lane); }
#pragma unroll
    for (int n = 0; n < 8; ++n) PIN4(wf[n]);
    v4u a0 = {0u, 0u, 0u, 0u}, a1 = a0;
    const int hsrc = 4 * ks5 + (w >> 1);
    const int o5 = WS_H5 + (i >> 1) * 16384 + (hsrc >> 4) * 8192 + (i & 1) * 4096 + (hsrc & 15) * 256 + (w & 1) * 128 + g4 * 16;
    for (unsigned sp = 0; sp < SPIN_CAP; ++sp) { RELOAD(); if (i < 8) { a0 = ld16(ws, o5); a1 = ld16(ws, o5 + 64); } if (__all(nos4(a0) & nos4(a1))) break; }
    STAMP(11);
#pragma unroll
    for (int mt = 0; mt < 4; ++mt) {
      f4 d = {0.f, 0.f, 0.f, 0.f};
      d = mfma(a0, wf[mt], d); d = mfma(a1, wf[4 + mt], d);
      const float v = sum16(d[0] + d[2], d[1] + d[3]);
      if (lane < 32) LDS_WRF(L_ARED + ((w * 2 + g4) * 64 + 16 * mt + i) * 4, v);
    }
    BAR();
    if (t < 64) {
      float s0_ = 0.f, s1_ = 0.f;
#pragma unroll
      for (int ww = 0; ww < 8; ++ww) { const v2u v = LDS_RD64(L_ARED + ((ww * 2 + (t >> 5)) * 64 + 2 * (t & 31)) * 4); s0_ += __uint_as_float(v.x); s1_ += __uint_as_float(v.y); }
      st4(ws, WS_H6 + (ks5 * 2 + (t >> 5)) * 1280 + ms * 128 + (t & 31) * 4, nosent(pk(s0_, s1_)));
    }
    STAMP(12);
  } else if (r7) {
#pragma unroll
    for (int n = 0; n < 6; ++n) { const int ins = 8 + w * 6 + n; if (ins < 54) dma_tile<32>(src7, NM * 2, L_CKV, ins, lane); }
    VMW(0);
    BAR();
    STAMP(10);
    v4u wf[7];
#pragma unroll
    for (int n = 0; n < 7; ++n) { const int ks = min(w + 8 * n, 53); wf[n] = trfrag<32>(lds, L_CKV, 32 * ks, 0, lane); }
#pragma unroll
    for (int n = 0; n < 7; ++n) PIN4(wf[n]);
    float x1v = 0.f, gbv = 0.f, b2v = 0.f;
    if (w == 0) {
      const int bb = (lane >> 4) & 1, m = 16 * mt7 + i;
      const float xv = blo(p.x[bb * NM + m]); b2v = blo(p.b2[m]);
      unsigned ga, gb, pa[8];
      for (unsigned sp = 0; sp < SPIN_CAP; ++sp) { RELOAD(); ga = ld4(ws, WS_MV + m * 4); gb = ld4(ws, WS_MV + (1920 + m) * 4); if (__all((ga != SENT) & (gb != SENT))) break; }
      for (unsigned sp = 0; sp < SPIN_CAP; ++sp) { RELOAD();
        bool ok = true;
#pragma unroll
        for (int k = 0; k < 8; ++k) pa[k] = ld4(ws, WS_H6 + (k * 2 + bb) * 1280 + (m >> 1) * 4);
#pragma unroll
        for (int k = 0; k < 8; ++k) ok &= pa[k] != SENT;
        if (__all(ok)) break;
      }
      float a = 0.f;
#pragma unroll
      for (int k = 0; k < 8; ++k) a += (m & 1) ? bhi(pa[k]) : blo(pa[k]);
      x1v = rbf(xv + rbf((bb ? bhi(ga) : blo(ga)) * rbf(a)));
      gbv = bb ? bhi(gb) : blo(gb);
    }
    STAMP(12);
    v4u af[7];
#pragma unroll
    for (int n = 0; n < 7; ++n) af[n] = (v4u){0u, 0u, 0u, 0u};
    const int o7 = WS_H7 + (i & 1) * (FF * 2) + (32 * w + 8 * g4) * 2;
    for (unsigned sp = 0; sp < SPIN_CAP; ++sp) { RELOAD();
      bool ok = true;
      if (i < 2) {
#pragma unroll
        for (int n = 0; n < 6; ++n) af[n] = ld16(ws, o7 + n * 512);
        if (w < 6) af[6] = ld16(ws, o7 + 6 * 512);
      }
#pragma unroll
      for (int n = 0; n < 7; ++n) ok &= nos4(af[n]);
      if (__all(ok)) break;
    }
    STAMP(13);
    f4 d = {0.f, 0.f, 0.f, 0.f};
#pragma unroll
    for (int n = 0; n < 7; ++n) d = mfma(af[n], wf[n], d);
    if (lane < 16) { LDS_WRF(L_ARED + (w * 32 + lane) * 4, d[0]); LDS_WRF(L_ARED + (w * 32 + 16 + lane) * 4, d[1]); }
    BAR();
    if (t < 32) {
      float a = 0.f;
#pragma unroll
      for (int ww = 0; ww < 8; ++ww) a += LDS_RDF(L_ARED + (ww * 32 + t) * 4);
      const float y = rbf(rbf(a) + b2v);
      p.out[(t >> 4) * NM + 16 * mt7 + (t & 15)] = (u16)pk(x1v + rbf(gbv * y), 0.f);
      if (mt7 == 0 && t == 0) st4(ws, WS_E, tag);
    }
  }
}

__device__ __forceinline__ void c_path(const Params& p, LAS unsigned char* lds, rsrc_t ws, int g, int w, int lane
) {
  const int t = threadIdx.x, xcd = g & 7, rc = (g >> 3) - 16, h = xcd * 4 + (rc >> 2), j = rc & 3;
  const int i = lane & 15, g4 = lane >> 4;
  const bool rmod = rc < 5;
  const int p6 = xcd + 8 * ((rc - 5) >> 1), t6 = 2 * p6 + ((rc - 5) & 1);
  const bool r6 = (rc >= 5) & (rc < 13) & (p6 < 27);
  const unsigned tag = ld4(ws, WS_E) + 1u;
  float ln0 = 0.f, ln1 = 0.f;
  v4u xa, xb, xc; unsigned xm = 0;
  if (w < 3) {
    const u16* xr = p.x + (lane >> 5) * NM + (lane & 31) * 8;
    xa = gld16(xr); xb = gld16(xr + 256); xc = gld16(xr + ((lane & 31) < 16 ? 512 : 0));
    xm = *(const GAS unsigned*)(p.x + (t & 1) * NM + 160 * j + min(t >> 1, 79) * 2);
  } else {
    const unsigned char* src = (const unsigned char*)p.wq + ((size_t)160 * j * HD + h * 128) * 2;
#pragma unroll
    for (int n = 0; n < 8; ++n) dma_tile<256>(src, HD * 2, L_WQ, (w - 3) * 8 + n, lane);
  }
  v4u uk[8];
  if (w < 4) {
#pragma unroll
    for (int T = 0; T < 2; ++T)
#pragma unroll
      for (int ks = 0; ks < 4; ++ks)
        uk[T * 4 + ks] = gld16nt(p.wuk + ((size_t)(128 * j + 32 * w + 8 * (i >> 2) + 4 * T + (i & 3)) * 32 + h) * 128 + 32 * ks + 8 * g4);
  }
  { const v4u sv = {SENT, SENT, SENT, SENT}; const int o = WS_H4 + (h * 4 + j) * 16384 + t * 16; st16(ws, o, sv); st16(ws, o + 8192, sv); }
  STAMP(1);
  if (w < 3) {
    const bool v3 = (lane & 31) < 16;
    float sm = 0.f;
#pragma unroll
    for (int k = 0; k < 4; ++k) sm += (blo(xa[k]) + bhi(xa[k])) + (blo(xb[k]) + bhi(xb[k])) + (v3 ? blo(xc[k]) + bhi(xc[k]) : 0.f);
    const float mean = rsum32(sm) * (1.f / NM);
    float sq = 0.f;
#pragma unroll
    for (int k = 0; k < 4; ++k) {
      float d;
      d = blo(xa[k]) - mean; sq += d * d; d = bhi(xa[k]) - mean; sq += d * d; d = blo(xb[k]) - mean; sq += d * d; d = bhi(xb[k]) - mean; sq += d * d;
      d = blo(xc[k]) - mean; sq += v3 ? d * d : 0.f; d = bhi(xc[k]) - mean; sq += v3 ? d * d : 0.f;
    }
    const float var = rsum32(sq) * (1.f / NM);
    const float mb = rbf(mean), sd = rbf(sqrtf(rbf(rbf(var) + 1e-5f)));
    const float mb0 = rdl(mb, 0), mb1 = rdl(mb, 32), sd0 = rdl(sd, 0), sd1 = rdl(sd, 32);
    const float m_ = (t & 1) ? mb1 : mb0, s_ = (t & 1) ? sd1 : sd0;
    ln0 = rbf(rbf(blo(xm) - m_) / s_); ln1 = rbf(rbf(bhi(xm) - m_) / s_);
    asm volatile("" : "+v"(ln0), "+v"(ln1));
  } else if (w == 3) { VMW(10); } else { VMW(2); }
  BAR();
  STAMP(2);
  v4u wf[5];
#pragma unroll
  for (int ks = 0; ks < 5; ++ks) wf[ks] = trfrag<256>(lds, L_WQ, 32 * ks, w, lane);
#pragma unroll
  for (int ks = 0; ks < 5; ++ks) PIN4(wf[ks]);
  if (w < 3) {
    const int wh = t & 1, o1 = WS_H1 + (wh * NM + 160 * j + min(t >> 1, 79) * 2) * 8;
    v4u v[5];
    for (unsigned sp = 0; sp < SPIN_CAP; ++sp) { RELOAD();
      bool ok = true;
#pragma unroll
      for (int k = 0; k < 5; ++k) v[k] = ld16(ws, o1 + k * 10240);
#pragma unroll
      for (int k = 0; k < 5; ++k) ok &= (v[k].y == tag) & (v[k].w == tag);
      if (__all(ok)) break;
    }
    float a00 = 0.f, a01 = 0.f, a10 = 0.f, a11 = 0.f;
#pragma unroll
    for (int k = 0; k < 5; ++k) { a00 += blo(v[k].x); a01 += bhi(v[k].x); a10 += blo(v[k].z); a11 += bhi(v[k].z); }
    const float keep0 = rbf(wh ? a01 : a00), keep1 = rbf(wh ? a11 : a10), send0 = rbf(wh ? a00 : a01), send1 = rbf(wh ? a10 : a11);
    const float rc0 = dpp<0xB1>(send0), rc1 = dpp<0xB1>(send1);
    const float sa0 = wh ? rc0 : keep0, sa1 = wh ? rc1 : keep1, ca0 = wh ? keep0 : rc0, ca1 = wh ? keep1 : rc1;
    const float h0 = rbf(ln0 * rbf(1.f + ca0)) + sa0, h1 = rbf(ln1 * rbf(1.f + ca1)) + sa1;
    if (t < 160) LDS_WR32(L_HS + wh * 320 + (t >> 1) * 4, pk(h0, h1));
  }
  BAR();
  STAMP(3);
  const int h2b = WS_H2 + (h & 15) * 16384 + (h >> 4) * 8192;
  {
    f4 d = {0.f, 0.f, 0.f, 0.f};
#pragma unroll
    for (int ks = 0; ks < 5; ++ks) d = mfma(wf[ks], LDS_RD128(L_HS + (i & 1) * 320 + (32 * ks + 8 * g4) * 2), d);
    if (i < 2) {
      const int o = h2b + (j * 8 + w) * 256 + (i * 4 + g4) * 16;
      st16(ws, o, (v4u){__float_as_uint(d[0]), tag, __float_as_uint(d[1]), tag});
      st16(ws, o + 128, (v4u){__float_as_uint(d[2]), tag, __float_as_uint(d[3]), tag});
    }
  }
  STAMP(4);
  {
    const int o = h2b + (g4 * 8 + w) * 256 + (lane & 15) * 16;
    v4u v;
    for (unsigned sp = 0; sp < SPIN_CAP; ++sp) { RELOAD(); v = ld16(ws, o); if (__all((v.y == tag) & (v.w == tag))) break; }
    float q0 = __uint_as_float(v.x), q1 = __uint_as_float(v.z);
    q0 = sum16(q0, q0); q0 = sum32(q0, q0); q1 = sum16(q1, q1); q1 = sum32(q1, q1);
    if (lane < 16) LDS_WR32(L_QH + ((lane >> 2) & 1) * 256 + (16 * w + 4 * (lane & 3) + 2 * (lane >> 3)) * 2, pk(q0, q1));
  }
  BAR();
  STAMP(5);
  if (w < 4) {
    f4 d0 = {0.f, 0.f, 0.f, 0.f}, d1 = d0;
#pragma unroll
    for (int ks = 0; ks < 4; ++ks) { const v4u qf = LDS_RD128(L_QH + (i & 1) * 256 + (32 * ks + 8 * g4) * 2); d0 = mfma(uk[ks], qf, d0); d1 = mfma(uk[4 + ks], qf, d1); }
    const float sc = 0.08838834764831845f * 1.4426950408889634f;
    if (i < 2) st16(ws, WS_H3 + (h >> 3) * 16384 + i * 8192 + (h & 7) * 1024 + (128 * j + 32 * w + 8 * g4) * 2,
                    (v4u){nosent(pk(d0[0] * sc, d0[1] * sc)), nosent(pk(d0[2] * sc, d0[3] * sc)), nosent(pk(d1[0] * sc, d1[1] * sc)), nosent(pk(d1[2] * sc, d1[3] * sc))});
  }
  STAMP(6);
  BAR();
  {
    const unsigned char* src = (const unsigned char*)p.wuv + ((size_t)128 * j * HD + h * 128) * 2;
#pragma unroll
    for (int n = 0; n < 4; ++n) dma_tile<256>(src, HD * 2, L_WUV, w * 4 + n, lane);
  }
  if (rmod) {
    const unsigned char* src = (const unsigned char*)p.wmod + (size_t)(1280 + 64 * (xcd * 5 + rc)) * 2;
#pragma unroll
    for (int n = 0; n < 10; ++n) dma_tile<128>(src, 7680, L_ROLE, w * 10 + n, lane);
    VMW(10);
  } else if (r6) {
    const unsigned char* src = (const unsigned char*)p.w1 + (size_t)32 * t6 * 2;
#pragma unroll
    for (int n = 0; n < 5; ++n) dma_tile<64>(src, FF * 2, L_ROLE, w * 5 + n, lane);
    VMW(5);
  } else { VMW(0); }
  BAR();
  STAMP(7);
  v4u uv[16];
#pragma unroll
  for (int ks = 0; ks < 2; ++ks)
#pragma unroll
    for (int nt = 0; nt < 8; ++nt) uv[ks * 8 + nt] = trfrag<256>(lds, L_WUV, 64 * (w & 1) + 32 * ks, nt, lane);
#pragma unroll
  for (int n = 0; n < 16; ++n) PIN4(uv[n]);
  v4u w1f[5];
  if (rmod) {
    if (t < 160) LDS_WR128(L_SC + t * 16, silu8(gld16(p.c + t * 8)));
    VMW(0);
    BAR();
    if (w < 4) {
      f4 d = {0.f, 0.f, 0.f, 0.f};
#pragma unroll
      for (int ks = 0; ks < 20; ++ks) d = mfma(LDS_RD128(L_SC + (i & 1) * 1280 + (32 * ks + 8 * g4) * 2), trfrag<128>(lds, L_ROLE, 32 * ks, w, lane), d);
      if (lane < 16) st4(ws, WS_MV + (64 * (xcd * 5 + rc) + 16 * w + lane) * 4, nosent(pk(d[0], d[1])));
    }
  } else if (r6) {
    VMW(0);
    BAR();
#pragma unroll
    for (int ks = 0; ks < 5; ++ks) w1f[ks] = trfrag<64>(lds, L_ROLE, 160 * (w >> 1) + 32 * ks, w & 1, lane);
#pragma unroll
    for (int ks = 0; ks < 5; ++ks) PIN4(w1f[ks]);
  }
  STAMP(8);
  {
    const int bb = w >> 2, sh = (w >> 1) & 1, kh = w & 1;
    const int os = WS_ST + bb * 8192 + ((h >> 4) * 32 + 16 * (sh ^ ((i >> 2) & 1)) + 4 * g4 + (i & 3)) * 128 + (h & 15) * 8;
    v2u sv = {0u, 0u};
    for (unsigned sp = 0; sp < SPIN_CAP; ++sp) { RELOAD(); if (i < 8) sv = ld8(ws, os); if (__all((sv.x != SENT) & (sv.y != SENT))) break; }
    const int oa = WS_H4 + (h * 4 + j) * 16384 + (bb ^ 1) * 8192 + (16 * sh + i) * 256 + kh * 128 + g4 * 16;
    v4u a0, a1;
    for (unsigned sp = 0; sp < SPIN_CAP; ++sp) { RELOAD(); a0 = ld16(ws, oa); a1 = ld16(ws, oa + 64); if (__all(nos4(a0) & nos4(a1))) break; }
    STAMP(9);
    const float mv = i < 8 ? __uint_as_float(sv.x) : -__builtin_inff(), lv = i < 8 ? __uint_as_float(sv.y) : 0.f;
    float M = fmaxf(mv, dpp<0xB1>(mv)); M = fmaxf(M, dpp<0x4E>(M)); M = fmaxf(M, dpp<0x141>(M)); M = fmaxf(M, dpp<0x140>(M)); M = max32(max16(M));
    const float e = __builtin_amdgcn_exp2f(mv - M);
    float L = e * lv; L += dpp<0xB1>(L); L += dpp<0x4E>(L); L += dpp<0x141>(L); L += dpp<0x140>(L); L = sum16(L, L); L = sum32(L, L);
    const float wg = e / L;
    const float w0 = dpp<0x150>(wg), w1_ = dpp<0x151>(wg), w2_ = dpp<0x152>(wg), w3_ = dpp<0x153>(wg);
    float o[8];
#pragma unroll
    for (int nt = 0; nt < 8; ++nt) {
      f4 d = {0.f, 0.f, 0.f, 0.f};
      d = mfma(a0, uv[nt], d); d = mfma(a1, uv[8 + nt], d);
      o[nt] = (w0 * d[0] + w1_ * d[1]) + (w2_ * d[2] + w3_ * d[3]);
    }
    const float u0 = sum32(o[0], o[4]), u1 = sum32(o[1], o[5]), u2 = sum32(o[2], o[6]), u3 = sum32(o[3], o[7]);
    const float v0 = sum16(u0, u2), v1 = sum16(u1, u3);
    LDS_WRF(L_CRED + (w * 128 + 32 * g4 + i) * 4, v0); LDS_WRF(L_CRED + (w * 128 + 32 * g4 + 16 + i) * 4, v1);
    BAR();
    if (t < 64) {
      const int b2_ = t >> 5, dq = t & 31;
      f4 a = __builtin_bit_cast(f4, LDS_RD128(L_CRED + ((b2_ * 4) * 128 + 4 * dq) * 4));
#pragma unroll
      for (int ww = 1; ww < 4; ++ww) a += __builtin_bit_cast(f4, LDS_RD128(L_CRED + ((b2_ * 4 + ww) * 128 + 4 * dq) * 4));
      st8(ws, WS_H5 + j * 16384 + (h >> 4) * 8192 + b2_ * 4096 + (h & 15) * 256 + dq * 8, (v2u){nosent(pk(a[0], a[1])), nosent(pk(a[2], a[3]))});
    }
    STAMP(10);
  }
  if (r6) {
    const int tq = min(t, 319), bb = tq >= 160, mq = tq - 160 * bb;
    float xv[4], gav[4], sbv[4], cbv[4];
    float b1a = 0.f, b1b = 0.f;
    if (w < 5) {
      const v2u xx = *(const GAS v2u*)(p.x + bb * NM + 4 * mq);
      if (t < 32) { const unsigned bv = *(const GAS unsigned*)(p.b1 + 32 * t6 + 2 * (t & 15)); b1a = blo(bv); b1b = bhi(bv); }
      v4u ga, sb, cbb;
      for (unsigned sp = 0; sp < SPIN_CAP; ++sp) { RELOAD(); ga = ld16(ws, WS_MV + mq * 16); sb = ld16(ws, WS_MV + 2560 + mq * 16); cbb = ld16(ws, WS_MV + 5120 + mq * 16); if (__all(nos4(ga) & nos4(sb) & nos4(cbb))) break; }
      xv[0] = blo(xx.x); xv[1] = bhi(xx.x); xv[2] = blo(xx.y); xv[3] = bhi(xx.y);
#pragma unroll
      for (int k = 0; k < 4; ++k) { gav[k] = bb ? bhi(ga[k]) : blo(ga[k]); sbv[k] = bb ? bhi(sb[k]) : blo(sb[k]); cbv[k] = rbf(1.f + (bb ? bhi(cbb[k]) : blo(cbb[k]))); }
#pragma unroll
      for (int k = 0; k < 4; ++k) asm volatile("" : "+v"(gav[k]), "+v"(sbv[k]), "+v"(cbv[k]), "+v"(xv[k]));
    }
    STAMP(11);
    float x1[4];
    if (w < 5) {
      v2u pa[8];
      for (unsigned sp = 0; sp < SPIN_CAP; ++sp) { RELOAD();
        bool ok = true;
#pragma unroll
        for (int k = 0; k < 8; ++k) pa[k] = ld8(ws, WS_H6 + (k * 2 + bb) * 1280 + mq * 8);
#pragma unroll
        for (int k = 0; k < 8; ++k) ok &= (pa[k].x != SENT) & (pa[k].y != SENT);
        if (__all(ok)) break;
      }
      STAMP(12);
      float a[4] = {0.f, 0.f, 0.f, 0.f};
      unsigned one_lo = 0x00003F80u, one_hi = 0x3F800000u;
      asm volatile("" : "+s"(one_lo), "+s"(one_hi));
#pragma unroll
      for (int k = 0; k < 8; ++k) { a[0] = dot2(pa[k].x, one_lo, a[0]); a[1] = dot2(pa[k].x, one_hi, a[1]); a[2] = dot2(pa[k].y, one_lo, a[2]); a[3] = dot2(pa[k].y, one_hi, a[3]); }
#pragma unroll
      for (int k = 0; k < 4; ++k) x1[k] = rbf(xv[k] + rbf(gav[k] * rbf(a[k])));
      float s1 = rsum32((x1[0] + x1[1]) + (x1[2] + x1[3])), s2 = rsum32((x1[0] * x1[0] + x1[1] * x1[1]) + (x1[2] * x1[2] + x1[3] * x1[3]));
      if ((lane & 31) == 0) LDS_WR64(L_CRED + (t >> 5) * 8, ((v2u){__float_as_uint(s1), __float_as_uint(s2)}));
    }
    BAR();
    if (w < 5) {
      float s1 = 0.f, s2 = 0.f;
#pragma unroll
      for (int k = 0; k < 5; ++k) { const v2u v = LDS_RD64(L_CRED + (bb * 5 + k) * 8); s1 += __uint_as_float(v.x); s2 += __uint_as_float(v.y); }
      const float mean = s1 * (1.f / NM), var = fmaxf(s2 * (1.f / NM) - mean * mean, 0.f);
      const float mb = rbf(mean), sd = rbf(sqrtf(rbf(rbf(var) + 1e-5f)));
      float hv[4];
#pragma unroll
      for (int k = 0; k < 4; ++k) hv[k] = rbf(rbf(rbf(x1[k] - mb) / sd) * cbv[k]) + sbv[k];
      LDS_WR64(L_SC + bb * 1280 + mq * 8, ((v2u){pk(hv[0], hv[1]), pk(hv[2], hv[3])}));
    }
    BAR();
    f4 d = {0.f, 0.f, 0.f, 0.f};
#pragma unroll
    for (int ks = 0; ks < 5; ++ks) d = mfma(LDS_RD128(L_SC + (i & 1) * 1280 + (160 * (w >> 1) + 32 * ks + 8 * g4) * 2), w1f[ks], d);
    if (lane < 16) { LDS_WRF(L_CRED + 128 + ((w >> 1) * 64 + (w & 1) * 16 + lane) * 4, d[0]); LDS_WRF(L_CRED + 128 + ((w >> 1) * 64 + 32 + (w & 1) * 16 + lane) * 4, d[1]); }
    BAR();
    if (t < 32) {
      float a0 = 0.f, a1 = 0.f;
#pragma unroll
      for (int k = 0; k < 4; ++k) { const v2u v = LDS_RD64(L_CRED + 128 + (k * 64 + 2 * t) * 4); a0 += __uint_as_float(v.x); a1 += __uint_as_float(v.y); }
      const float r0 = fmaxf(rbf(rbf(a0) + b1a), 0.f), r1 = fmaxf(rbf(rbf(a1) + b1b), 0.f);
      st4(ws, WS_H7 + (t >> 4) * (FF * 2) + (32 * t6 + 2 * (t & 15)) * 2, nosent(pk(r0 * r0, r1 * r1)));
    }
    STAMP(13);
  }
}

__global__ void __launch_bounds__(512, 2) fused_decode(Params p) {
  extern __shared__ __attribute__((aligned(16))) unsigned char lds_raw[];
  LAS unsigned char* lds = (LAS unsigned char*)lds_raw;
  const int g = blockIdx.x, w = __builtin_amdgcn_readfirstlane(threadIdx.x >> 6), lane = threadIdx.x & 63;
  const rsrc_t ws = __builtin_amdgcn_make_buffer_rsrc(p.ws, 0, WS_BYTES, 0x00020000);
  if ((g >> 3) < 16) a_path(p, lds, ws, g, w, lane); else c_path(p, lds, ws, g, w, lane);
}

extern "C" void kernel_launch(void* const* d_in, const int* in_sizes, int n_in,
                              void* d_out, int out_size, void* d_ws, size_t ws_size,
                              hipStream_t stream) {
  Params p;
  p.x = (const u16*)d_in[0]; p.ckv = (const u16*)d_in[1]; p.wq = (const u16*)d_in[2]; p.wuk = (const u16*)d_in[3];
  p.wuv = (const u16*)d_in[4]; p.wo = (const u16*)d_in[5]; p.w1 = (const u16*)d_in[7]; p.b1 = (const u16*)d_in[8];
  p.w2 = (const u16*)d_in[9]; p.b2 = (const u16*)d_in[10]; p.c = (const u16*)d_in[11]; p.wmod = (const u16*)d_in[12];
  p.out = (u16*)d_out; p.ws = (unsigned char*)d_ws;
  (void)hipFuncSetAttribute((const void*)fused_decode, hipFuncAttributeMaxDynamicSharedMemorySize, LDS_BYTES);
  hipLaunchKernelGGL(fused_decode, dim3(256), dim3(512), LDS_BYTES, stream, p);
}
```

```cpp
#include <hip/hip_runtime.h>
#include <hip/hip_bf16.h>
#include <stdint.h>

#define LAS __attribute__((address_space(3)))
#define GAS __attribute__((address_space(1)))
typedef unsigned short u16;
typedef unsigned v4u __attribute__((ext_vector_type(4)));
typedef unsigned v2u __attribute__((ext_vector_type(2)));
typedef float f4 __attribute__((ext_vector_type(4)));
typedef float f2 __attribute__((ext_vector_type(2)));
typedef short s8v __attribute__((ext_vector_type(8)));
typedef short s4v __attribute__((ext_vector_type(4)));
typedef __bf16 bf2v __attribute__((ext_vector_type(2)));
typedef unsigned short us8v __attribute__((ext_vector_type(8)));
typedef __amdgpu_buffer_rsrc_t rsrc_t;

constexpr int SK = 4096, NM = 640, DC = 512, FF = 1728, HD = 4096;

#define INFL1 6
#define NSLOW 4
#define N37 8
#define DLY_CLR 8
#define INFL 6
#define UKPF 1
#define DLY_PF 4
#define N3 8
#define DLY_HELP 28
#define CODE_LEN 20480
#define DLY_WQ 6
#define DLY_UK 8
#define DLY_CKV 8

constexpr int WS_E = 0;
constexpr int WS_PR = 256;
constexpr int WS_CF = 4096;
constexpr int WS_H1 = 16384;
constexpr int WS_H2 = 180224;
constexpr int WS_H3 = 442368;
constexpr int WS_ST = 507904;
constexpr int WS_H5 = 524288;
constexpr int WS_H6 = 589824;
constexpr int WS_H7 = 655360;
constexpr int WS_MV = 671744;
constexpr int WS_CLR = WS_H3;
constexpr int WS_H4 = 2097152;
constexpr int WS_BYTES = 16 << 20;
constexpr int STAMP_OFF = 8 << 20;
constexpr unsigned SENT = 0xFFFFFFFFu;
constexpr unsigned SPIN_CAP = 30000u;

constexpr int L_CKV = 0, L_QS = 131072, L_AM = 147456;
constexpr int L_W2 = 81920;
constexpr int L_MX = L_AM, L_LS = L_AM + 256, L_PB = L_AM + 512, L_ARED = L_AM + 4608, L_ASC = L_AM + 8704;
constexpr int L_WQ = 0, L_WUV = 0, L_UK = 40960, L_ROLE = 40960, L_CM = 122880;
constexpr int L_HS = L_CM, L_QH = L_CM + 640, L_SC = L_CM + 1280, L_CRED = L_CM + 3840, L_GS = L_CM + 8192, L_MU = L_CM + 1152;
constexpr int LDS_BYTES = 163840, L_SCR = LDS_BYTES - 512, L_FLIP = LDS_BYTES - 528;

#define STAMP(i) do {} while (0)
#define STAMPW(i, w) do {} while (0)
#define ST_ARG
#define ST_PASS

struct Params {
  const u16 *x, *ckv, *wq, *wuk, *wuv, *wo, *w1, *b1, *w2, *b2, *c, *wmod;
  u16* out;
  unsigned char* ws;
};

__device__ __forceinline__ float blo(unsigned w) { return __uint_as_float(w << 16); }
__device__ __forceinline__ float bhi(unsigned w) { return __uint_as_float(w & 0xffff0000u); }
__device__ __forceinline__ unsigned pk(float lo, float hi) { f2 f = {lo, hi}; return __builtin_bit_cast(unsigned, __builtin_convertvector(f, bf2v)); }
__device__ __forceinline__ float rbf(float f) { return blo(pk(f, 0.f)); }
__device__ __forceinline__ unsigned nosent(unsigned v) { return v < 0xFFFFFFFEu ? v : 0xFFFFFFFEu; }
__device__ __forceinline__ f4 mfma(v4u a, v4u b, f4 c) { return __builtin_amdgcn_mfma_f32_16x16x32_bf16(__builtin_bit_cast(s8v, a), __builtin_bit_cast(s8v, b), c, 0, 0, 0); }
template <int C> __device__ __forceinline__ float dpp(float x) { return __builtin_bit_cast(float, __builtin_amdgcn_mov_dpp(__builtin_bit_cast(int, x), C, 0xf, 0xf, true)); }
template <int C> __device__ __forceinline__ unsigned dppu(unsigned x) { return (unsigned)__builtin_amdgcn_mov_dpp((int)x, C, 0xf, 0xf, true); }
__device__ __forceinline__ float sum16(float a, float b) { auto s = __builtin_amdgcn_permlane16_swap(__float_as_uint(a), __float_as_uint(b), false, false); return __uint_as_float(s[0]) + __uint_as_float(s[1]); }
__device__ __forceinline__ float sum32(float a, float b) { auto s = __builtin_amdgcn_permlane32_swap(__float_as_uint(a), __float_as_uint(b), false, false); return __uint_as_float(s[0]) + __uint_as_float(s[1]); }
__device__ __forceinline__ float vmax(float a, float b) { return __builtin_elementwise_maximum(a, b); }
__device__ __forceinline__ float vmax3(float a, float b, float c) { return vmax(vmax(a, b), c); }
__device__ __forceinline__ float max16(float a) { auto s = __builtin_amdgcn_permlane16_swap(__float_as_uint(a), __float_as_uint(a), false, false); return vmax(__uint_as_float(s[0]), __uint_as_float(s[1])); }
__device__ __forceinline__ float max32(float a) { auto s = __builtin_amdgcn_permlane32_swap(__float_as_uint(a), __float_as_uint(a), false, false); return vmax(__uint_as_float(s[0]), __uint_as_float(s[1])); }
__device__ __forceinline__ float rsum32(float v) {
  v += dpp<0xB1>(v); v += dpp<0x4E>(v); v += dpp<0x141>(v); v += dpp<0x140>(v); return sum16(v, v);
}
__device__ __forceinline__ float rdl(float v, int l) { return __builtin_bit_cast(float, __builtin_amdgcn_readlane(__builtin_bit_cast(int, v), l)); }
__device__ __forceinline__ float dot2(unsigned a, unsigned b, float c) { return __builtin_amdgcn_fdot2_f32_bf16(__builtin_bit_cast(bf2v, a), __builtin_bit_cast(bf2v, b), c, false); }

__device__ __forceinline__ rsrc_t mkrs(const void* p, int bytes) { return __builtin_amdgcn_make_buffer_rsrc((void*)p, 0, bytes, 0x00020000); }
__device__ __forceinline__ v4u ld16(rsrc_t r, int off) { return __builtin_amdgcn_raw_buffer_load_b128(r, off, 0, 16); }
__device__ __forceinline__ v2u ld8(rsrc_t r, int off) { return __builtin_amdgcn_raw_buffer_load_b64(r, off, 0, 16); }
__device__ __forceinline__ unsigned ld4(rsrc_t r, int off) { return __builtin_amdgcn_raw_buffer_load_b32(r, off, 0, 16); }
__device__ __forceinline__ void st16(rsrc_t r, int off, v4u v) { __builtin_amdgcn_raw_buffer_store_b128(v, r, off, 0, 16); }
__device__ __forceinline__ void st8(rsrc_t r, int off, v2u v) { __builtin_amdgcn_raw_buffer_store_b64(v, r, off, 0, 16); }
__device__ __forceinline__ void st4(rsrc_t r, int off, unsigned v) { __builtin_amdgcn_raw_buffer_store_b32(v, r, off, 0, 16); }
__device__ __forceinline__ v4u ld16s(rsrc_t r, int off, int so) { return __builtin_amdgcn_raw_buffer_load_b128(r, off, so, 16); }
__device__ __forceinline__ void st16s(rsrc_t r, int off, int so, v4u v) { __builtin_amdgcn_raw_buffer_store_b128(v, r, off, so, 16); }
__device__ __forceinline__ v4u gld16(const void* p) { return *(const GAS v4u*)p; }
__device__ __forceinline__ unsigned ldE(rsrc_t r) { return __builtin_amdgcn_raw_buffer_load_b32(r, WS_E, 0, 0); }
__device__ __forceinline__ void dma(rsrc_t r, int voff, int soff, int lds_dst) {
  asm volatile("s_mov_b32 m0, %0\n\ts_nop 0\n\tbuffer_load_dwordx4 %1, %2, %3 offen nt lds" :: "s"(lds_dst), "v"(voff), "s"(r), "s"(soff) : "m0", "memory");
}
__device__ __forceinline__ void dma_t(rsrc_t r, int voff, int soff, int lds_dst) {
  asm volatile("s_mov_b32 m0, %0\n\ts_nop 0\n\tbuffer_load_dwordx4 %1, %2, %3 offen lds" :: "s"(lds_dst), "v"(voff), "s"(r), "s"(soff) : "m0", "memory");
}
__device__ __forceinline__ void l2pf(rsrc_t r, int voff, int scr) {
  asm volatile("s_mov_b32 m0, %0\n\ts_nop 0\n\tbuffer_load_dword %1, %2, 0 offen lds" :: "s"(scr), "v"(voff), "s"(r) : "m0", "memory");
}
__device__ __forceinline__ void l2pf_nt(rsrc_t r, int voff, int scr) {
  asm volatile("s_mov_b32 m0, %0\n\ts_nop 0\n\tbuffer_load_dword %1, %2, 0 offen nt lds" :: "s"(scr), "v"(voff), "s"(r) : "m0", "memory");
}
#define BAR() asm volatile("s_waitcnt lgkmcnt(0)\n\ts_barrier" ::: "memory")
#define VMW(n) asm volatile("s_waitcnt vmcnt(%0)" :: "n"(n) : "memory")
#define RELOAD() asm volatile("" ::: "memory")
#define PIN(v) asm volatile("" : "+v"(v))
#define SCHED() __builtin_amdgcn_sched_barrier(0)
__device__ __forceinline__ unsigned mx4(v4u v) { return max(max(v.x, v.y), max(v.z, v.w)); }
__device__ __forceinline__ bool nos4(v4u v) { return mx4(v) != SENT; }
__device__ __forceinline__ int fsw(int k) { return (k & 3) ^ ((k & 4) ? 12 : 0) ^ ((k & 8) ? 4 : 0); }

#define LDS_RD128(off) (*(const LAS v4u*)(lds + (off)))
#define LDS_RD64(off) (*(const LAS v2u*)(lds + (off)))
#define LDS_RDF(off) (*(const LAS float*)(lds + (off)))
#define LDS_WR128(off, v) (*(LAS v4u*)(lds + (off)) = (v))
#define LDS_WR64(off, v) (*(LAS v2u*)(lds + (off)) = (v))
#define LDS_WR32(off, v) (*(LAS unsigned*)(lds + (off)) = (v))
#define LDS_WRF(off, v) (*(LAS float*)(lds + (off)) = (v))

template <int RB> __device__ __forceinline__ int swz(int row) {
  if (RB == 256) return 2 * ((row & 3) | (((row >> 3) & 1) << 2));
  if (RB == 128) return 2 * (((row >> 1) & 1) | (((row >> 3) & 1) << 1));
  if (RB == 64) return 2 * ((row >> 3) & 1);
  return 0;
}
__device__ __forceinline__ v2u trrd(LAS unsigned char* lds, int off) { return __builtin_bit_cast(v2u, __builtin_amdgcn_ds_read_tr16_b64_v4i16((LAS s4v*)(lds + off))); }
template <int RB> __device__ __forceinline__ int tr_lane(int ctile, int lane) {
  const int g4 = lane >> 4, q = (lane & 15) >> 2, p = lane & 3, r0 = 8 * g4 + q;
  if (RB == 32) return (r0 ^ ((g4 & 1) << 2)) * 32 + p * 8;
  return r0 * RB + (((2 * ctile + (p >> 1)) ^ swz<RB>(r0)) << 4) + (p & 1) * 8;
}
template <int RB> __device__ __forceinline__ v4u trfrag(LAS unsigned char* lds, int off) {
  v2u a = trrd(lds, off), b = trrd(lds, RB == 32 ? (off ^ 128) : off + 4 * RB);
  return (v4u){a.x, a.y, b.x, b.y};
}
template <int RB> __device__ __forceinline__ int dma_lane(int ld, int lane, int par) {
  constexpr int CPR = RB / 16;
  const int r = lane / CPR, pos = lane % CPR;
  if (RB == 32) return (r ^ (((r >> 3) & 1) << 2)) * ld + pos * 16;
  return r * ld + ((pos ^ swz<RB>(r + 8 * par)) << 4);
}
__device__ __forceinline__ v4u silu8(v4u c) {
  v4u o;
#pragma unroll
  for (int i = 0; i < 4; ++i) { float a = blo(c[i]), b = bhi(c[i]); o[i] = pk(a * __builtin_amdgcn_rcpf(1.f + __expf(-a)), b * __builtin_amdgcn_rcpf(1.f + __expf(-b))); }
  return o;
}
__device__ __forceinline__ v4u quad_gather(unsigned v) { return (v4u){dppu<0x00>(v), dppu<0x55>(v), dppu<0xAA>(v), dppu<0xFF>(v)}; }

__device__ __forceinline__ unsigned poll_votes(rsrc_t ws, unsigned tag, int lane) {
  const int off = WS_CF + (lane >> 5) * 8192 + min(lane & 31, 27) * 128;
  unsigned long long ones = 0;
  for (unsigned sp = 0; sp < SPIN_CAP; ++sp) {
    RELOAD();
    const v4u v = ld16(ws, off);
    const unsigned long long ok = __ballot(v.x == tag), ok2 = __ballot(v.z == tag);
    ones = __ballot((v.x == tag) & (v.y == 1u));
    if ((((unsigned)ok | (unsigned)(ok >> 32)) & ((unsigned)ok2 | (unsigned)(ok2 >> 32))) == 0xFFFFFFFFu) break;
  }
  return __builtin_popcount(((unsigned)ones | (unsigned)(ones >> 32)) & 0x0FFFFFFFu) >= 14 ? 1u : 0u;
}

#define UK_PREFETCH() l2pf(mkrs(p.wuk, DC * HD * 2), ((g >> 3) * 32 + (w - 4) * 8 + (lane >> 3)) * HD * 2 + (g & 7) * 1024 + (lane & 7) * 128, L_SCR)
#define UK_N 1
__device__ __forceinline__ void a_path(const Params& p, LAS unsigned char* lds, rsrc_t ws, int g, int w, int lane, unsigned tag ST_ARG) {
  const int t = threadIdx.x, xcd = g & 7, r = g >> 3, b = xcd >> 2, s = (xcd & 3) * 8 + (r >> 1), hg = r & 1;
  const int i = lane & 15, g4 = lane >> 4;
  const bool s0 = g < 100;
  const rsrc_t rk = mkrs(p.ckv + ((size_t)b * SK + s * 128) * DC, 131072);
  const int l16 = lane << 4, rot = hg * 64;
#define CKV_ROW(k_) do { const int kk_ = (k_); dma(rk, l16 ^ (fsw(kk_ & 15) << 4), ((kk_ + rot) & 127) * 1024, L_CKV + kk_ * 1024); } while (0)
  if (w >= 4) { __builtin_amdgcn_s_sleep(DLY_PF); UK_PREFETCH(); }
  __builtin_amdgcn_s_sleep(DLY_CKV);
#pragma unroll 1
  for (int n = 0; n < NSLOW; ++n) { VMW(INFL1 - 1); CKV_ROW(n * 8 + w); }
#pragma unroll 1
  for (int n = NSLOW; n < 16; ++n) { VMW(INFL - 1); CKV_ROW(n * 8 + w); }
  if (w == 0) { const unsigned f = poll_votes(ws, tag, lane); if (lane == 0) LDS_WR32(L_FLIP, f); }
  VMW(INFL < 8 ? INFL : 8);
  BAR();
  STAMP(3);
  const int fl = __builtin_amdgcn_readfirstlane(*(const LAS unsigned*)(lds + L_FLIP)), myc = (b ^ fl) * 8192;
  int h4o = WS_H4 + ((4 * g4 * 4 + (w & 3)) * 2 + b) * 16384 + (hg ^ fl ^ 1) * 8192 + s * 256 + i * 16;
  {
    const v4u sv = {SENT, SENT, SENT, SENT};
    st16(ws, h4o + (w >> 2) * 262144, sv); st16(ws, h4o + (w >> 2) * 262144 + 131072, sv);
  }
  v4u fr[32];
  int qa[4];
#pragma unroll
  for (int k4 = 0; k4 < 4; ++k4) qa[k4] = i * 1024 + ((((4 * k4 + g4) ^ fsw(i))) << 4);
#pragma unroll
  for (int hf = 0; hf < 2; ++hf) {
    if (hf == 1) {
      VMW(2);
      BAR();
      STAMP(4);
    }
    if (w < 4) {
#pragma unroll
      for (int ks = 0; ks < 16; ++ks) fr[hf * 16 + ks] = LDS_RD128(L_CKV + (64 * hf + 16 * w) * 1024 + qa[ks & 3] + (ks >> 2) * 256);
    } else {
      const int pw = w - 4;
#pragma unroll
      for (int u = 0; u < 4; ++u) {
        v4u X[4];
#pragma unroll
        for (int jj = 0; jj < 4; ++jj) { const int kl = 4 * g4 + jj; X[jj] = LDS_RD128(L_CKV + (64 * hf + 16 * u + kl) * 1024 + pw * 256 + ((i ^ fsw(kl)) << 4)); }
#pragma unroll
        for (int tt = 0; tt < 8; ++tt)
#pragma unroll
          for (int m = 0; m < 2; ++m) fr[u * 8 + tt][2 * hf + m] = __builtin_amdgcn_perm(X[2 * m + 1][tt >> 1], X[2 * m][tt >> 1], (tt & 1) ? 0x07060302u : 0x05040100u);
      }
    }
    if (hf == 0) {
#pragma unroll
      for (int n = 0; n < 32; ++n) PIN(fr[n]);
    }
  }
#pragma unroll
  for (int n = 0; n < 32; ++n) PIN(fr[n]);
#pragma unroll
  for (int k4 = 0; k4 < 4; ++k4) { qa[k4] += L_QS; PIN(qa[k4]); }
  const bool r5 = r < 10, r7 = (r >= 10) & (r < 15);
  const int mt7 = xcd * 5 + (r - 10);
  const rsrc_t rr = r5 ? mkrs(p.wo, HD * NM * 2) : mkrs(p.wmod, NM * 3840 * 2);
  const int rld = r5 ? NM * 2 : 7680;
  const int rl0 = dma_lane<128>(rld, lane, 0), rl1 = dma_lane<128>(rld, lane, 1);
  const int rso = r5 ? (512 * xcd * NM + 64 * r) * 2 : (1280 + 64 * mt7) * 2;
#define ROLE_INS(n_) do { const int in_ = (n_); dma(rr, (in_ & 1) ? rl1 : rl0, rso + in_ * 8 * rld, L_CKV + in_ * 1024); } while (0)
  int mxw = L_MX + i * 16 + (w & 3) * 4, mxr = L_MX + i * 16, pbw = L_PB + (w & 3) * 1024 + l16, pbr = L_PB + l16;
  PIN(h4o); PIN(mxw); PIN(mxr); PIN(pbw); PIN(pbr);
  STAMP(5);
  {
    const int o0 = WS_H3 + (hg * 2) * 16384 + myc + t * 16;
    const int d0 = L_QS + w * 1024 + ((lane ^ fsw(w)) << 4), d1 = L_QS + (w + 8) * 1024 + ((lane ^ fsw(w + 8)) << 4);
    v4u q0, q1;
    for (unsigned sp = 0; sp < SPIN_CAP; ++sp) { RELOAD(); q0 = ld16(ws, o0); q1 = ld16(ws, o0 + 16384); if (__all(max(mx4(q0), mx4(q1)) != SENT)) break; }
    LDS_WR128(d0, q0); LDS_WR128(d1, q1);
  }
  BAR();
  STAMP(6);
  if (w < 4) {
    f4 sacc[2];
    sacc[0] = (f4){0.f, 0.f, 0.f, 0.f}; sacc[1] = sacc[0];
    v4u qf[16];
#pragma unroll
    for (int ks = 0; ks < 12; ++ks) qf[ks] = LDS_RD128(qa[ks & 3] + (ks >> 2) * 256);
    SCHED();
#pragma unroll
    for (int ks = 0; ks < 4; ++ks) { sacc[0] = mfma(fr[ks], qf[ks], sacc[0]); sacc[1] = mfma(fr[16 + ks], qf[ks], sacc[1]); qf[12 + ks] = LDS_RD128(qa[ks] + 768); }
    SCHED();
#pragma unroll
    for (int ks = 4; ks < 16; ++ks) { sacc[0] = mfma(fr[ks], qf[ks], sacc[0]); sacc[1] = mfma(fr[16 + ks], qf[ks], sacc[1]); }
    float mx = vmax3(vmax3(sacc[0][0], sacc[0][1], sacc[0][2]), vmax3(sacc[0][3], sacc[1][0], sacc[1][1]), vmax(sacc[1][2], sacc[1][3]));
    mx = __builtin_ceilf(max32(max16(mx)));
    LDS_WRF(mxw, mx);
    float pp[8];
#pragma unroll
    for (int n = 0; n < 8; ++n) pp[n] = __builtin_amdgcn_exp2f(sacc[n >> 2][n & 3] - mx);
    LDS_WR128(pbw, ((v4u){pk(pp[0], pp[1]), pk(pp[2], pp[3]), pk(pp[4], pp[5]), pk(pp[6], pp[7])}));
    STAMP(7);
  } else {
    if (r5 | r7) {
    const int n3 = r5 ? 4 * N3 : 4 * N37;
#pragma unroll 1
    for (int n = w - 4; n < n3; n += 4) ROLE_INS(n);
    }
  }
  BAR();
  if (w == 0 || w >= 4) {
    const f4 m4 = __builtin_bit_cast(f4, LDS_RD128(mxr));
    v4u pf[4];
#pragma unroll
    for (int u = 0; u < 4; ++u) pf[u] = LDS_RD128(pbr + u * 1024);
    SCHED();
    const float mrow = vmax(vmax(m4[0], m4[1]), vmax(m4[2], m4[3]));
#pragma unroll
    for (int u = 0; u < 4; ++u) {
      const unsigned dd = min((unsigned)(mrow - m4[u]), 255u) * 0x00800080u;
      pf[u] = __builtin_bit_cast(v4u, __builtin_elementwise_sub_sat(__builtin_bit_cast(us8v, pf[u]), __builtin_bit_cast(us8v, (v4u){dd, dd, dd, dd})));
    }
    if (w == 0) {
      const v4u ones = {0x3F803F80u, 0x3F803F80u, 0x3F803F80u, 0x3F803F80u};
      f4 l4 = {0.f, 0.f, 0.f, 0.f};
#pragma unroll
      for (int u = 0; u < 4; ++u) l4 = mfma(ones, pf[u], l4);
      if (lane < 16) st8(ws, WS_ST + (hg ^ fl) * 8192 + (b * 32 + s) * 128 + lane * 8, (v2u){nosent(__float_as_uint(mrow)), nosent(__float_as_uint(l4[0]))});
      STAMP(8);
    } else {
      f4 o[8];
      v4u ov[4];
#pragma unroll
      for (int tp = 0; tp < 4; ++tp) {
        o[2 * tp] = (f4){0.f, 0.f, 0.f, 0.f}; o[2 * tp + 1] = o[2 * tp];
#pragma unroll
        for (int u = 0; u < 4; ++u) { o[2 * tp] = mfma(pf[u], fr[u * 8 + 2 * tp], o[2 * tp]); o[2 * tp + 1] = mfma(pf[u], fr[u * 8 + 2 * tp + 1], o[2 * tp + 1]); }
        if (tp > 0) {
#pragma unroll
          for (int rg = 0; rg < 4; ++rg) ov[rg][tp - 1] = nosent(pk(o[2 * tp - 2][rg], o[2 * tp - 1][rg]));
        }
        SCHED();
      }
#pragma unroll
      for (int rg = 0; rg < 4; ++rg) { ov[rg][3] = nosent(pk(o[6][rg], o[7][rg])); st16(ws, h4o + rg * 131072, ov[rg]); }
      STAMPW(9, 4);
      VMW(0);
      STAMPW(20, 4);
    }
  } else if (r7) {
    v4u cc;
    asm volatile("buffer_load_dwordx4 %0, %1, %2, 0 offen\n\ts_waitcnt vmcnt(0)" : "=&v"(cc) : "v"((t - 64) * 16), "s"(mkrs(p.c, 2560)) : "memory");
    LDS_WR128(L_ASC + (t - 64) * 16, silu8(cc));
  }
  BAR();
  if (r5) {
#pragma unroll 1
    for (int n = 4 * N3 + w; n < 64; n += 8) ROLE_INS(n);
    VMW(0);
    BAR();
    STAMP(10);
    v4u wf[8];
#pragma unroll
    for (int mt = 0; mt < 4; ++mt) {
      const int a = L_CKV + 64 * w * 128 + tr_lane<128>(mt, lane);
      wf[mt] = trfrag<128>(lds, a); wf[4 + mt] = trfrag<128>(lds, a + 4096);
    }
#pragma unroll
    for (int n = 0; n < 8; ++n) PIN(wf[n]);
    v4u a0 = {0u, 0u, 0u, 0u}, a1 = a0;
    const int hsrc = 4 * xcd + (w >> 1);
    const int o5 = WS_H5 + (i >> 1) * 16384 + myc + (i & 1) * 4096 + (hsrc & 15) * 256 + (w & 1) * 128 + g4 * 16;
    int rw = L_ARED + ((w * 2 + g4) * 64 + i) * 4, rd = L_ARED + ((t >> 5) * 64 + 2 * (t & 31)) * 4;
    int o6 = WS_H6 + (xcd >> 1) * 16384 + (xcd & 1) * 4096 + ((t >> 5) & 1) * 1280 + r * 128 + (t & 28) * 4 + (t & 1) * 8192;
    PIN(rw); PIN(rd); PIN(o6);
    for (unsigned sp = 0; sp < SPIN_CAP; ++sp) { RELOAD(); if (i < 8) { a0 = ld16(ws, o5); a1 = ld16(ws, o5 + 64); } if (__all(max(mx4(a0), mx4(a1)) != SENT)) break; }
    STAMP(11);
    f4 d[4];
#pragma unroll
    for (int mt = 0; mt < 4; ++mt) d[mt] = mfma(a0, wf[mt], (f4){0.f, 0.f, 0.f, 0.f});
#pragma unroll
    for (int mt = 0; mt < 4; ++mt) d[mt] = mfma(a1, wf[4 + mt], d[mt]);
#pragma unroll
    for (int mt = 0; mt < 4; ++mt) {
      const float v = sum16(d[mt][0] + d[mt][2], d[mt][1] + d[mt][3]);
      if (lane < 32) LDS_WRF(rw + 64 * mt, v);
    }
    BAR();
    if (w == 0) {
      v2u v[8];
#pragma unroll
      for (int ww = 0; ww < 8; ++ww) v[ww] = LDS_RD64(rd + ww * 512);
      const float s0_ = ((__uint_as_float(v[0].x) + __uint_as_float(v[1].x)) + (__uint_as_float(v[2].x) + __uint_as_float(v[3].x))) + ((__uint_as_float(v[4].x) + __uint_as_float(v[5].x)) + (__uint_as_float(v[6].x) + __uint_as_float(v[7].x)));
      const float s1_ = ((__uint_as_float(v[0].y) + __uint_as_float(v[1].y)) + (__uint_as_float(v[2].y) + __uint_as_float(v[3].y))) + ((__uint_as_float(v[4].y) + __uint_as_float(v[5].y)) + (__uint_as_float(v[6].y) + __uint_as_float(v[7].y)));
      const v4u o = quad_gather(nosent(pk(s0_, s1_)));
      if ((lane & 3) < 2) st16(ws, o6, o);
    }
    STAMP(12);
  } else if (r7) {
#pragma unroll 1
    for (int n = 4 * N37 + w; n < 80; n += 8) ROLE_INS(n);
    VMW(0);
    BAR();
    STAMP(19);
    {
      f4 d = {0.f, 0.f, 0.f, 0.f};
      const int a = L_CKV + (w >> 2) * 40960 + tr_lane<128>(w & 3, lane), sa = L_ASC + (i & 1) * 1280 + (w >> 2) * 640 + g4 * 16;
      v4u af[10], bf[10];
#pragma unroll
      for (int ks = 0; ks < 10; ++ks) { af[ks] = LDS_RD128(sa + ks * 64); bf[ks] = trfrag<128>(lds, a + ks * 4096); }
      f4 d2 = d;
#pragma unroll
      for (int ks = 0; ks < 5; ++ks) { d = mfma(af[2 * ks], bf[2 * ks], d); d2 = mfma(af[2 * ks + 1], bf[2 * ks + 1], d2); }
      d += d2;
      if (w >= 4 && lane < 16) LDS_WR64(L_ARED + ((w - 4) * 16 + lane) * 8, ((v2u){__float_as_uint(d[0]), __float_as_uint(d[1])}));
      BAR();
      if (w < 4) {
        const v2u o = LDS_RD64(L_ARED + (w * 16 + i) * 8);
        const int n = 64 * mt7 + 16 * w + i;
        if (lane < 32) st4(ws, WS_MV + (n >= 1920 ? 16384 + (n - 1920) * 4 : n * 4) + g4 * 8192, nosent(pk(d[0] + __uint_as_float(o.x), d[1] + __uint_as_float(o.y))));
        VMW(0);
      }
      STAMP(11);
      BAR();
    }
    {
      const rsrc_t r2 = mkrs(p.w2, FF * NM * 2);
      const int l2 = dma_lane<32>(NM * 2, lane, 0), rot2 = (mt7 & 3) * 14;
#pragma unroll 1
      for (int n = w; n < 54; n += 8) { int in = n + rot2; if (in >= 54) in -= 54; dma(r2, l2, 32 * mt7 + in * 32 * NM * 2, L_W2 + in * 1024); }
    }
    VMW(0);
    BAR();
    STAMP(10);
    v4u wf[7];
    {
      const int a = L_W2 + w * 1024 + tr_lane<32>(0, lane);
#pragma unroll
      for (int n = 0; n < 6; ++n) wf[n] = trfrag<32>(lds, a + n * 8192);
      wf[6] = trfrag<32>(lds, a + (w < 6 ? 6 * 8192 : 0));
    }
#pragma unroll
    for (int n = 0; n < 7; ++n) PIN(wf[n]);
    float x1v = 0.f, gbv = 0.f, b2v = 0.f;
    if (w == 0) {
      const int bb = g4 & 1, m = 16 * mt7 + i;
      const float xv = blo(p.x[bb * NM + m]); b2v = blo(p.b2[m]);
      unsigned ga, gb, pa[8];
      for (unsigned sp = 0; sp < SPIN_CAP; ++sp) { RELOAD(); ga = ld4(ws, WS_MV + myc + m * 4); gb = ld4(ws, WS_MV + 16384 + myc + m * 4); if (__all((ga != SENT) & (gb != SENT))) break; }
      for (unsigned sp = 0; sp < SPIN_CAP; ++sp) {
        RELOAD();
        bool ok = true;
#pragma unroll
        for (int k = 0; k < 8; ++k) pa[k] = ld4(ws, WS_H6 + (k >> 1) * 16384 + myc + (k & 1) * 4096 + bb * 1280 + (m >> 1) * 4);
#pragma unroll
        for (int k = 0; k < 8; ++k) ok &= pa[k] != SENT;
        if (__all(ok)) break;
      }
      float a = 0.f;
#pragma unroll
      for (int k = 0; k < 8; ++k) a += (m & 1) ? bhi(pa[k]) : blo(pa[k]);
      x1v = rbf(xv + rbf((bb ? bhi(ga) : blo(ga)) * rbf(a)));
      gbv = bb ? bhi(gb) : blo(gb);
      PIN(x1v); PIN(gbv); PIN(b2v);
    }
    STAMP(12);
    v4u af[7];
#pragma unroll
    for (int n = 0; n < 7; ++n) af[n] = (v4u){0u, 0u, 0u, 0u};
    const int o7 = WS_H7 + myc + w * 128 + (i & 1) * 64 + g4 * 16, o76 = o7 + (w < 6 ? 6 * 1024 : 0);
    for (unsigned sp = 0; sp < SPIN_CAP; ++sp) {
      RELOAD();
      if (i < 2) {
#pragma unroll
        for (int n = 0; n < 6; ++n) af[n] = ld16(ws, o7 + n * 1024);
        af[6] = ld16(ws, o76);
      }
      unsigned mm = 0;
#pragma unroll
      for (int n = 0; n < 7; ++n) mm = max(mm, mx4(af[n]));
      if (__all(mm != SENT)) break;
    }
    STAMP(13);
    f4 d = {0.f, 0.f, 0.f, 0.f}, d2 = d;
#pragma unroll
    for (int n = 0; n < 3; ++n) { d = mfma(af[2 * n], wf[2 * n], d); d2 = mfma(af[2 * n + 1], wf[2 * n + 1], d2); }
    if (w < 6) d = mfma(af[6], wf[6], d);
    if (lane < 16) { LDS_WRF(L_ARED + (w * 32 + lane) * 4, d[0] + d2[0]); LDS_WRF(L_ARED + (w * 32 + 16 + lane) * 4, d[1] + d2[1]); }
    BAR();
    if (t < 32) {
      float v[8];
#pragma unroll
      for (int ww = 0; ww < 8; ++ww) v[ww] = LDS_RDF(L_ARED + (ww * 32 + t) * 4);
      const float a = ((v[0] + v[1]) + (v[2] + v[3])) + ((v[4] + v[5]) + (v[6] + v[7]));
      const float y = rbf(rbf(a) + b2v);
      p.out[(t >> 4) * NM + 16 * mt7 + (t & 15)] = (u16)pk(x1v + rbf(gbv * y), 0.f);
      if (mt7 == 0 && t == 0) st4(ws, WS_E, tag);
    }
  }
}

__device__ __forceinline__ void c_path(const Params& p, LAS unsigned char* lds, rsrc_t ws, int g, int w, int lane ST_ARG) {
  const int t = threadIdx.x, xcd = g & 7, rc = (g >> 3) - 16, h = xcd * 4 + (rc >> 2), j = rc & 3, iod = xcd >> 2;
  const int i = lane & 15, g4 = lane >> 4;
  const int p6 = xcd + 8 * (rc >> 1), t6 = 2 * p6 + (rc & 1);
  const bool r6 = p6 < 27;
  const unsigned tag = ldE(ws) + 1u;
  float ln0 = 0.f, ln1 = 0.f;
  v4u xa, xb, xc; unsigned xm = 0;
  const int dl0 = dma_lane<256>(HD * 2, lane, 0), dl1 = dma_lane<256>(HD * 2, lane, 1);
  if (w < 3) {
    const u16* xr = p.x + (lane >> 5) * NM + (lane & 31) * 8;
    xa = gld16(xr); xb = gld16(xr + 256); xc = gld16(xr + ((lane & 31) < 16 ? 512 : 0));
    xm = *(const GAS unsigned*)(p.x + (t & 1) * NM + 160 * j + min(t >> 1, 79) * 2);
  } else {
    const rsrc_t rq = mkrs(p.wq, NM * HD * 2);
    const int so = (160 * j * HD + h * 128) * 2 + (w - 3) * 32 * HD * 2;
    __builtin_amdgcn_s_sleep(DLY_WQ);
#pragma unroll
    for (int n = 0; n < 8; ++n) dma(rq, (n & 2) ? dl1 : dl0, so + n * 4 * HD * 2, L_WQ + (w - 3) * 8192 + n * 1024);
  }
  STAMP(1);
  if (w < 3) {
    const bool v3 = (lane & 31) < 16;
    float sm = 0.f;
#pragma unroll
    for (int k = 0; k < 4; ++k) sm += (blo(xa[k]) + bhi(xa[k])) + (blo(xb[k]) + bhi(xb[k])) + (v3 ? blo(xc[k]) + bhi(xc[k]) : 0.f);
    const float mean = rsum32(sm) * (1.f / NM);
    float sq = 0.f;
#pragma unroll
    for (int k = 0; k < 4; ++k) {
      float d;
      d = blo(xa[k]) - mean; sq += d * d; d = bhi(xa[k]) - mean; sq += d * d; d = blo(xb[k]) - mean; sq += d * d; d = bhi(xb[k]) - mean; sq += d * d;
      d = blo(xc[k]) - mean; sq += v3 ? d * d : 0.f; d = bhi(xc[k]) - mean; sq += v3 ? d * d : 0.f;
    }
    const float var = rsum32(sq) * (1.f / NM);
    const float mb = rbf(mean), sd = rbf(sqrtf(rbf(rbf(var) + 1e-5f)));
    const float mb0 = rdl(mb, 0), mb1 = rdl(mb, 32), sd0 = rdl(sd, 0), sd1 = rdl(sd, 32);
    const float m_ = (t & 1) ? mb1 : mb0, s_ = (t & 1) ? sd1 : sd0;
    ln0 = rbf(rbf(blo(xm) - m_) / s_); ln1 = rbf(rbf(bhi(xm) - m_) / s_);
    PIN(ln0); PIN(ln1);
    if ((t & 223) == 0) LDS_WRF(L_MU + (t >> 5) * 4, mean);
  }
  if (w >= 3) VMW(0);
  BAR();
  STAMP(2);
  v4u wf[5];
  {
    const int a = L_WQ + tr_lane<256>(w, lane);
#pragma unroll
    for (int ks = 0; ks < 5; ++ks) wf[ks] = trfrag<256>(lds, a + ks * 8192);
  }
#pragma unroll
  for (int ks = 0; ks < 5; ++ks) PIN(wf[ks]);
  const int h2b = WS_H2 + (h & 15) * 16384;
  int hsr = L_HS + (i & 1) * 320 + g4 * 16, h2o = h2b + (j * 8 + w) * 256 + (i * 4 + g4) * 16, h2i = h2b + (g4 * 8 + w) * 256 + i * 16;
  int qhw = L_QH + ((lane >> 2) & 1) * 256 + (16 * w + 64 * (lane >> 5) + 4 * (lane & 3) + 2 * ((lane >> 3) & 1) + (g4 & 1)) * 2, qhr = L_QH + (i & 1) * 256 + g4 * 16;
  int h3o = WS_H3 + (h >> 3) * 16384 + (i & 1) * 8192 + (h & 7) * 1024 + (128 * j + 32 * w + 8 * g4) * 2;
  PIN(hsr); PIN(h2o); PIN(h2i); PIN(qhw); PIN(qhr); PIN(h3o);
  unsigned one_lo = 0x00003F80u, one_hi = 0x3F800000u;
  asm volatile("" : "+s"(one_lo), "+s"(one_hi));
  if (w < 3) {
    const int wh = t & 1, o1 = WS_H1 + (wh * NM + 160 * j + min(t >> 1, 79) * 2) * 8;
    int hsw = L_HS + wh * 320 + min(t >> 1, 79) * 4; PIN(hsw);
    const unsigned shk = wh ? 0u : 16u, shs = wh ? 16u : 0u;
    float k0, k1, s0, s1;
    for (unsigned sp = 0; sp < SPIN_CAP; ++sp) {
      RELOAD();
      v4u v[5];
      unsigned bad = 0;
#pragma unroll
      for (int k = 0; k < 5; ++k) v[k] = ld16(ws, o1 + k * 10240);
      k0 = k1 = s0 = s1 = 0.f;
#pragma unroll
      for (int k = 0; k < 5; ++k) {
        bad |= (v[k].y ^ tag) | (v[k].w ^ tag);
        s0 += bhi(v[k].x << shs); s1 += bhi(v[k].z << shs); k0 += bhi(v[k].x << shk); k1 += bhi(v[k].z << shk);
      }
      PIN(k0); PIN(k1); PIN(s0); PIN(s1);
      if (__all(bad == 0u)) break;
    }
    STAMP(16);
    const float rc0 = dpp<0xB1>(s0), rc1 = dpp<0xB1>(s1);
    const float sa0 = wh ? rc0 : k0, sa1 = wh ? rc1 : k1, ca0 = wh ? k0 : rc0, ca1 = wh ? k1 : rc1;
    LDS_WR32(hsw, pk(fmaf(ln0, ca0, ln0 + sa0), fmaf(ln1, ca1, ln1 + sa1)));
  }
  if (w == 3) {
    const unsigned f = poll_votes(ws, tag, lane);
    if (lane == 0) LDS_WR32(L_FLIP, f);
    STAMPW(17, 3);
  }
  BAR();
  STAMP(3);
  int fl, myc;
  {
    v4u hf[5];
#pragma unroll
    for (int ks = 0; ks < 5; ++ks) hf[ks] = LDS_RD128(hsr + ks * 64);
    const unsigned flv = *(const LAS unsigned*)(lds + L_FLIP);
    SCHED();
    f4 d = {0.f, 0.f, 0.f, 0.f}, d2 = d;
    d = mfma(wf[0], hf[0], d); d2 = mfma(wf[1], hf[1], d2); d = mfma(wf[2], hf[2], d); d2 = mfma(wf[3], hf[3], d2); d = mfma(wf[4], hf[4], d);
    SCHED();
    fl = __builtin_amdgcn_readfirstlane(flv); myc = (iod ^ fl) * 8192;
    d += d2;
    if (i < 2) {
      st16s(ws, h2o, myc, (v4u){__float_as_uint(d[0]), tag, __float_as_uint(d[1]), tag});
      st16s(ws, h2o + 128, myc, (v4u){__float_as_uint(d[2]), tag, __float_as_uint(d[3]), tag});
    }
  }
  STAMP(4);
  int uka = L_UK + (32 * (w & 3) + 8 * (i >> 2) + (i & 3)) * 256 + ((g4 ^ i) << 4);
  PIN(uka);
  h3o ^= fl << 13;
  if (w >= 4) {
    const rsrc_t ru = mkrs(p.wuk, DC * HD * 2);
    const int so = (128 * j * HD + h * 128) * 2 + (w - 4) * 32 * HD * 2, ul = g4 * HD * 2 + ((i ^ g4) << 4);
#pragma unroll
    for (int n = 0; n < 8; ++n) dma(ru, ul ^ ((n >> 1) << 6), so + n * 4 * HD * 2, L_UK + (w - 4) * 8192 + n * 1024);
    VMW(0);
    STAMPW(18, 4);
  } else {
    v4u v, v2;
    for (unsigned sp = 0; sp < SPIN_CAP; ++sp) { RELOAD(); v = ld16s(ws, h2i, myc); v2 = ld16s(ws, h2i + 1024, myc); if (__all((v.y == tag) & (v.w == tag) & (v2.y == tag) & (v2.w == tag))) break; }
    STAMP(19);
    const float sc = 0.08838834764831845f * 1.4426950408889634f;
    const float q0 = sum16(__uint_as_float(v.x), __uint_as_float(v.z)), q1 = sum16(__uint_as_float(v2.x), __uint_as_float(v2.z));
    *(LAS u16*)(lds + qhw) = (u16)pk(sum32(q0, q1) * sc, 0.f);
  }
  BAR();
  STAMP(5);
  if (w < 4) {
    v4u qf[4], uk[8];
#pragma unroll
    for (int ks = 0; ks < 4; ++ks) { qf[ks] = LDS_RD128(qhr + ks * 64); uk[ks] = LDS_RD128(uka ^ (ks << 6)); uk[4 + ks] = LDS_RD128((uka ^ (ks << 6)) + 1024); }
    SCHED();
    f4 d0 = {0.f, 0.f, 0.f, 0.f}, d1 = d0;
#pragma unroll
    for (int ks = 0; ks < 4; ++ks) { d0 = mfma(uk[ks], qf[ks], d0); d1 = mfma(uk[4 + ks], qf[ks], d1); }
    if (i < 2) st16(ws, h3o, (v4u){nosent(pk(d0[0], d0[1])), nosent(pk(d0[2], d0[3])), nosent(pk(d1[0], d1[1])), nosent(pk(d1[2], d1[3]))});
  }
  STAMP(6);
  BAR();
  {
    const rsrc_t rv = mkrs(p.wuv, DC * HD * 2);
    const int so = (128 * j * HD + h * 128) * 2 + w * 16 * HD * 2;
#pragma unroll
    for (int n = 0; n < 4; ++n) dma(rv, (n & 2) ? dl1 : dl0, so + n * 4 * HD * 2, L_WUV + w * 4096 + n * 1024);
  }
  if (r6) {
    const rsrc_t r1 = mkrs(p.w1, NM * FF * 2);
    const int a0 = dma_lane<64>(FF * 2, lane, 0);
#pragma unroll 1
    for (int n = 0; n < 5; ++n) { int in = n * 8 + w + (t6 & 1) * 20; if (in >= 40) in -= 40; dma_t(r1, a0, 64 * t6 + in * 16 * FF * 2, L_ROLE + in * 1024); }
    VMW(5);
  } else if (rc >= 8) {
    const int u = ((rc - 8) * 8 + w) * 50 + lane;
    l2pf(mkrs(p.wmod, NM * 3840 * 2), lane < 50 ? (u / 5) * 7680 + 2560 + xcd * 640 + (u % 5) * 128 : 0x7ffffff0, L_SCR);
    VMW(1);
  } else { VMW(0); }
  BAR();
  STAMP(7);
  v4u uv[16];
#pragma unroll
  for (int nt = 0; nt < 8; ++nt) {
    const int a = L_WUV + (w & 1) * 16384 + tr_lane<256>(nt, lane);
    uv[nt] = trfrag<256>(lds, a); uv[8 + nt] = trfrag<256>(lds, a + 8192);
  }
#pragma unroll
  for (int n = 0; n < 16; ++n) PIN(uv[n]);
  v4u w1f[5];
  const int tq = min(t, 319), bb6 = tq >= 160, mq = tq - 160 * bb6;
  v2u xx = {0u, 0u}; unsigned b1v = 0;
  if (r6) {
    xx = *(const GAS v2u*)(p.x + bb6 * NM + 4 * mq);
    b1v = *(const GAS unsigned*)(p.b1 + 32 * t6 + 2 * (t & 15));
    VMW(0);
    BAR();
    const int a = L_ROLE + 160 * (w >> 1) * 64 + tr_lane<64>(w & 1, lane);
#pragma unroll
    for (int ks = 0; ks < 5; ++ks) w1f[ks] = trfrag<64>(lds, a + ks * 2048);
#pragma unroll
    for (int ks = 0; ks < 5; ++ks) PIN(w1f[ks]);
    PIN(xx); PIN(b1v);
  }
  STAMP(8);
  {
    const int bb = w >> 2, sh = (w >> 1) & 1, kh = w & 1;
    const int os = WS_ST + myc + (bb * 32 + 16 * (sh ^ ((i >> 2) & 1)) + 4 * g4 + (i & 3)) * 128 + (h & 15) * 8;
    const int oa = WS_H4 + (((h & 15) * 4 + j) * 2 + bb) * 16384 + (myc ^ 8192) + (16 * sh + i) * 256 + kh * 128 + g4 * 16;
    int rw = L_CRED + (w * 128 + 32 * g4 + i) * 4, rd = L_CRED + ((t >> 5) * 512 + 4 * (t & 31)) * 4;
    int o5 = WS_H5 + j * 16384 + myc + ((t >> 5) & 1) * 4096 + (h & 15) * 256 + (t & 31) * 8;
    PIN(rw); PIN(rd); PIN(o5);
    v2u sv = {0u, 0u};
    v4u a0, a1;
    for (unsigned sp = 0; sp < SPIN_CAP; ++sp) { RELOAD(); if (i < 8) sv = ld8(ws, os); if (__all((sv.x != SENT) & (sv.y != SENT))) break; }
    STAMP(20);
    for (unsigned sp = 0; sp < SPIN_CAP; ++sp) { RELOAD(); a0 = ld16(ws, oa); a1 = ld16(ws, oa + 64); if (__all(max(mx4(a0), mx4(a1)) != SENT)) break; }
    STAMP(9);
    f4 d[8];
#pragma unroll
    for (int nt = 0; nt < 8; ++nt) d[nt] = mfma(a0, uv[nt], (f4){0.f, 0.f, 0.f, 0.f});
    const float mv = i < 8 ? __uint_as_float(sv.x) : -__builtin_inff(), lv = i < 8 ? __uint_as_float(sv.y) : 0.f;
    float M = vmax(mv, dpp<0xB1>(mv)); M = vmax(M, dpp<0x4E>(M)); M = vmax(M, dpp<0x141>(M)); M = vmax(M, dpp<0x140>(M)); M = max32(max16(M));
    const float e = __builtin_amdgcn_exp2f(mv - M);
    float L = e * lv; L += dpp<0xB1>(L); L += dpp<0x4E>(L); L += dpp<0x141>(L); L += dpp<0x140>(L); L = sum16(L, L); L = sum32(L, L);
    const float wg = e * __builtin_amdgcn_rcpf(L);
    const float w0 = dpp<0x150>(wg), w1_ = dpp<0x151>(wg), w2_ = dpp<0x152>(wg), w3_ = dpp<0x153>(wg);
    float o[8];
#pragma unroll
    for (int nt = 0; nt < 8; ++nt) d[nt] = mfma(a1, uv[8 + nt], d[nt]);
#pragma unroll
    for (int nt = 0; nt < 8; ++nt) o[nt] = (w0 * d[nt][0] + w1_ * d[nt][1]) + (w2_ * d[nt][2] + w3_ * d[nt][3]);
    const float u0 = sum32(o[0], o[4]), u1 = sum32(o[1], o[5]), u2 = sum32(o[2], o[6]), u3 = sum32(o[3], o[7]);
    const float v0 = sum16(u0, u2), v1 = sum16(u1, u3);
    LDS_WRF(rw, v0); LDS_WRF(rw + 64, v1);
    BAR();
    if (w == 0) {
      const f4 a = (__builtin_bit_cast(f4, LDS_RD128(rd)) + __builtin_bit_cast(f4, LDS_RD128(rd + 512))) + (__builtin_bit_cast(f4, LDS_RD128(rd + 1024)) + __builtin_bit_cast(f4, LDS_RD128(rd + 1536)));
      st8(ws, o5, (v2u){nosent(pk(a[0], a[1])), nosent(pk(a[2], a[3]))});
    }
    STAMP(10);
  }
  if (rc >= 8) {
    const int row = ((rc - 8) * 8 + w) * 27 + (lane >> 1);
    if (w != 0) __builtin_amdgcn_s_sleep(6);
    l2pf(mkrs(p.w2, FF * NM * 2), (lane < 54) ? row * NM * 2 + ((160 * xcd) & ~127) + (lane & 1) * 128 : 0x7ffffff0, L_SCR);
  }
  if (r6) {
    const int bb = bb6;
    float xv[4], gav[4], cbv[4];
    float b1a = blo(b1v), b1b = bhi(b1v);
    if (w < 5) {
      v4u ga, sb, cbb;
      const int om = WS_MV + myc + mq * 16;
      for (unsigned sp = 0; sp < SPIN_CAP; ++sp) { RELOAD(); ga = ld16(ws, om); sb = ld16(ws, om + 2560); cbb = ld16(ws, om + 5120); if (__all(nos4(ga) & nos4(sb) & nos4(cbb))) break; }
      STAMP(22);
      const float mu0 = LDS_RDF(L_MU + bb * 4);
      xv[0] = blo(xx.x) - mu0; xv[1] = bhi(xx.x) - mu0; xv[2] = blo(xx.y) - mu0; xv[3] = bhi(xx.y) - mu0;
      float sbv[4];
#pragma unroll
      for (int k = 0; k < 4; ++k) { gav[k] = bb ? bhi(ga[k]) : blo(ga[k]); sbv[k] = bb ? bhi(sb[k]) : blo(sb[k]); cbv[k] = rbf(1.f + (bb ? bhi(cbb[k]) : blo(cbb[k]))); }
      LDS_WR64(L_GS + bb * 1280 + mq * 8, ((v2u){pk(cbv[0], cbv[1]), pk(cbv[2], cbv[3])}));
      LDS_WR64(L_GS + 2560 + bb * 1280 + mq * 8, ((v2u){pk(sbv[0], sbv[1]), pk(sbv[2], sbv[3])}));
    }
    BAR();
    {
      f4 d = {0.f, 0.f, 0.f, 0.f};
      v4u gf[5];
#pragma unroll
      for (int ks = 0; ks < 5; ++ks) gf[ks] = LDS_RD128(L_GS + (i & 3) * 1280 + 320 * (w >> 1) + g4 * 16 + ks * 64);
      SCHED();
#pragma unroll
      for (int ks = 0; ks < 5; ++ks) d = mfma(gf[ks], w1f[ks], d);
      if (lane < 16) {
#pragma unroll
        for (int q = 0; q < 4; ++q) LDS_WRF(L_GS + 5120 + (((w >> 1) * 4 + q) * 32 + (w & 1) * 16 + lane) * 4, d[q]);
      }
    }
    const int o6 = WS_H6 + myc + bb * 1280 + mq * 8;
    int scw = L_SC + bb * 1280 + mq * 8, scr = L_SC + (i & 1) * 1280 + 320 * (w >> 1) + g4 * 16, str = L_CRED + ((t >> 4) & 1) * 64;
    int stw = L_CRED + bb * 64 + ((t >> 5) - 5 * bb) * 8, rw = L_CRED + 128 + ((w >> 1) * 64 + (w & 1) * 16 + i) * 4, rd = L_CRED + 128 + 2 * (t & 31) * 4;
    int o7 = WS_H7 + 128 * t6 + (t & 28) * 4 + (t & 1) * 8192;
    PIN(scw); PIN(scr); PIN(str); PIN(o7); PIN(stw); PIN(rw); PIN(rd); PIN(b1a); PIN(b1b);
    int gsr = L_GS + 5120 + (((t >> 4) & 1) * 32 + 2 * (t & 15)) * 4; PIN(gsr);
#pragma unroll
    for (int k = 0; k < 4; ++k) { PIN(gav[k]); PIN(cbv[k]); PIN(xv[k]); }
#pragma unroll
    for (int ks = 0; ks < 5; ++ks) PIN(w1f[ks]);
    STAMP(11);
    float z[4] = {0.f, 0.f, 0.f, 0.f};
    if (w < 5) {
      float a[4];
      for (unsigned sp = 0; sp < SPIN_CAP; ++sp) {
        RELOAD();
        v2u pa[8];
        unsigned mm = 0;
#pragma unroll
        for (int k = 0; k < 8; ++k) pa[k] = ld8(ws, o6 + (k >> 1) * 16384 + (k & 1) * 4096);
        a[0] = a[1] = a[2] = a[3] = 0.f;
#pragma unroll
        for (int k = 0; k < 8; ++k) { mm = max(mm, max(pa[k].x, pa[k].y)); a[0] += blo(pa[k].x); a[1] += bhi(pa[k].x); a[2] += blo(pa[k].y); a[3] += bhi(pa[k].y); }
        PIN(a[0]); PIN(a[1]); PIN(a[2]); PIN(a[3]);
        if (__all(mm != SENT)) break;
      }
      STAMP(12);
#pragma unroll
      for (int k = 0; k < 4; ++k) z[k] = fmaf(gav[k], a[k], xv[k]);
      LDS_WR64(scw, ((v2u){pk(z[0] * cbv[0], z[1] * cbv[1]), pk(z[2] * cbv[2], z[3] * cbv[3])}));
    }
    BAR();
    v4u hf[5];
#pragma unroll
    for (int ks = 0; ks < 5; ++ks) hf[ks] = LDS_RD128(scr + ks * 64);
    v2u gv[4], sv[4];
#pragma unroll
    for (int k = 0; k < 4; ++k) { gv[k] = LDS_RD64(gsr + k * 512); sv[k] = LDS_RD64(gsr + k * 512 + 256); }
    SCHED();
    const float s1 = rsum32((z[0] + z[1]) + (z[2] + z[3])), s2 = rsum32((z[0] * z[0] + z[1] * z[1]) + (z[2] * z[2] + z[3] * z[3]));
    f4 d = {0.f, 0.f, 0.f, 0.f}, d2 = d;
    d = mfma(hf[0], w1f[0], d); d2 = mfma(hf[1], w1f[1], d2); d = mfma(hf[2], w1f[2], d); d2 = mfma(hf[3], w1f[3], d2); d = mfma(hf[4], w1f[4], d);
    const float gs0 = (__uint_as_float(gv[0].x) + __uint_as_float(gv[1].x)) + (__uint_as_float(gv[2].x) + __uint_as_float(gv[3].x));
    const float gs1 = (__uint_as_float(gv[0].y) + __uint_as_float(gv[1].y)) + (__uint_as_float(gv[2].y) + __uint_as_float(gv[3].y));
    b1a += (__uint_as_float(sv[0].x) + __uint_as_float(sv[1].x)) + (__uint_as_float(sv[2].x) + __uint_as_float(sv[3].x));
    b1b += (__uint_as_float(sv[0].y) + __uint_as_float(sv[1].y)) + (__uint_as_float(sv[2].y) + __uint_as_float(sv[3].y));
    if (w < 5 && (lane & 31) == 0) LDS_WR64(stw, ((v2u){__float_as_uint(s1), __float_as_uint(s2)}));
    if (lane < 16) { LDS_WRF(rw, d[0] + d2[0]); LDS_WRF(rw + 128, d[1] + d2[1]); }
    BAR();
    if (w == 0) {
      v2u v[4];
      const f4 sa = __builtin_bit_cast(f4, LDS_RD128(str)), sbq = __builtin_bit_cast(f4, LDS_RD128(str + 16));
      const v2u sc_ = LDS_RD64(str + 32);
#pragma unroll
      for (int k = 0; k < 4; ++k) v[k] = LDS_RD64(rd + k * 256);
      SCHED();
      const float dl = (((sa[0] + sa[2]) + (sbq[0] + sbq[2])) + __uint_as_float(sc_.x)) * (1.f / NM);
      const float q2 = (((sa[1] + sa[3]) + (sbq[1] + sbq[3])) + __uint_as_float(sc_.y)) * (1.f / NM);
      const float rs = __builtin_amdgcn_rsqf(fmaxf(fmaf(-dl, dl, q2), 0.f) + 1e-5f);
      const float a0 = (__uint_as_float(v[0].x) + __uint_as_float(v[1].x)) + (__uint_as_float(v[2].x) + __uint_as_float(v[3].x));
      const float a1 = (__uint_as_float(v[0].y) + __uint_as_float(v[1].y)) + (__uint_as_float(v[2].y) + __uint_as_float(v[3].y));
      const float r0 = fmaxf(fmaf(rs, fmaf(-dl, gs0, a0), b1a), 0.f), r1 = fmaxf(fmaf(rs, fmaf(-dl, gs1, a1), b1b), 0.f);
      const v4u o = quad_gather(nosent(pk(r0 * r0, r1 * r1)));
      if (t < 32 && (t & 3) < 2) st16(ws, o7, o);
    }
    STAMP(13);
  }
}

__global__ void __launch_bounds__(512, 2) fused_decode(Params p) {
  extern __shared__ __attribute__((aligned(16))) unsigned char lds_raw[];
  LAS unsigned char* lds = (LAS unsigned char*)lds_raw;
  const int g = blockIdx.x, w = __builtin_amdgcn_readfirstlane(threadIdx.x >> 6), lane = threadIdx.x & 63;
  const rsrc_t ws = __builtin_amdgcn_make_buffer_rsrc(p.ws, 0, WS_BYTES, 0x00020000);
  const bool s0 = g < 100;
  const int cb = g % 20, ks0 = g / 20, t = threadIdx.x;
  unsigned tag0 = 0;
  if (s0) {
    const int so = (128 * ks0 * 3840 + 64 * cb) * 2 + w * 16 * 7680;
    const rsrc_t rm = mkrs(p.wmod, NM * 3840 * 2);
    if (w < 4) {
      unsigned e0, cv;
      int s0a = L_QS + tr_lane<128>(w, lane), s0s = L_PB + (lane & 1) * 256 + (lane >> 4) * 16, h1o = WS_H1 + ks0 * 10240 + (64 * cb + 16 * w + lane) * 8;
      asm volatile("buffer_load_ushort %1, %3, %4, %5 offen\n\tbuffer_load_dword %0, off, %2, 0\n\t"
                   "s_mov_b32 m0, %6\n\ts_nop 0\n\tbuffer_load_dwordx4 %7, %8, %9 offen nt lds\n\t"
                   "s_mov_b32 m0, %10\n\ts_nop 0\n\tbuffer_load_dwordx4 %11, %8, %12 offen nt lds"
                   : "=&v"(e0), "=&v"(cv)
                   : "s"(ws), "v"(((t >> 7) * NM + (t & 127)) * 2), "s"(mkrs(p.c, 2560)), "s"(256 * ks0),
                     "s"(L_QS + w * 2048), "v"(dma_lane<128>(7680, lane, 0)), "s"(rm), "s"(so),
                     "s"(L_QS + w * 2048 + 1024), "v"(dma_lane<128>(7680, lane, 1)), "s"(so + 8 * 7680)
                   : "m0", "memory");
      asm volatile("s_waitcnt vmcnt(2)" : "+v"(e0), "+v"(cv), "+v"(s0a), "+v"(s0s), "+v"(h1o) :: "memory");
      STAMP(1);
      tag0 = e0 + 1u;
      const float a = blo(cv);
      *(LAS u16*)(lds + L_PB + t * 2) = (u16)pk(a * __builtin_amdgcn_rcpf(1.f + __expf(-a)), 0.f);
      VMW(0);
      STAMP(16);
      BAR();
      STAMP(2);
      f4 acc = {0.f, 0.f, 0.f, 0.f}, acc2 = acc;
      v4u bf[4], sa[4];
#pragma unroll
      for (int ks = 0; ks < 4; ++ks) { bf[ks] = trfrag<128>(lds, s0a + ks * 4096); sa[ks] = LDS_RD128(s0s + ks * 64); }
      SCHED();
      acc = mfma(sa[0], bf[0], acc); acc2 = mfma(sa[1], bf[1], acc2); acc = mfma(sa[2], bf[2], acc); acc2 = mfma(sa[3], bf[3], acc2);
      if (lane < 16) st8(ws, h1o, (v2u){pk(acc[0] + acc2[0], acc[1] + acc2[1]), tag0});
      STAMP(17);
    } else {
      dma(rm, dma_lane<128>(7680, lane, 0), so, L_QS + w * 2048);
      dma(rm, dma_lane<128>(7680, lane, 1), so + 8 * 7680, L_QS + w * 2048 + 1024);
      VMW(0);
      BAR();
    }
  } else if (g < 128) {
    if (w == 0) tag0 = ldE(ws) + 1u;
    if (w == 1) {
      const v4u sv = {SENT, SENT, SENT, SENT};
#pragma unroll 1
      for (int n = 0; n < 10; ++n) { const int ch = (g - 100) + 28 * n; if (ch < 256) st16(ws, WS_CLR + ch * 1024 + lane * 16, sv); }
      const unsigned tg = ldE(ws) + 1u;
      VMW(0);
      if (lane < 2) st8(ws, WS_CF + lane * 8192 + (g - 100) * 128 + 8, (v2u){tg, 0u});
      STAMPW(23, 1);
    } else if (w == 2) {
      unsigned tg;
      const int pa = WS_PR + (g - 100) * 128;
      unsigned long long t0, t1, t2, t3;
      if (lane == 0) { st4(ws, pa, 0u); st4(ws, pa + 8192, 0u); }
      asm volatile("s_waitcnt vmcnt(1)\n\ts_memtime %0\n\ts_waitcnt vmcnt(0)\n\ts_memtime %1\n\ts_waitcnt lgkmcnt(0)" : "=&s"(t0), "=&s"(t1) :: "memory");
      if (lane == 0) { st4(ws, pa + 8192 + 64, 0u); st4(ws, pa + 64, 0u); }
      asm volatile("buffer_load_dword %2, off, %3, 0\n\ts_waitcnt vmcnt(2)\n\ts_memtime %0\n\ts_waitcnt vmcnt(1)\n\ts_memtime %1\n\ts_waitcnt vmcnt(0) lgkmcnt(0)" : "=&s"(t2), "=&s"(t3), "=&v"(tg) : "s"(ws) : "memory");
      const unsigned vote = (((unsigned)(t1 - t0) > (unsigned)(t3 - t2)) ? 0u : 1u) ^ ((g >> 2) & 1);
      if (lane < 2) st8(ws, WS_CF + lane * 8192 + (g - 100) * 128, (v2u){tg + 1u, vote});
      STAMPW(22, 2);
    }
    BAR();
  }
  if ((g >> 3) < 16) a_path(p, lds, ws, g, w, lane, tag0 ST_PASS); else c_path(p, lds, ws, g, w, lane ST_PASS);
}

extern "C" void kernel_launch(void* const* d_in, const int* in_sizes, int n_in,
                              void* d_out, int out_size, void* d_ws, size_t ws_size,
                              hipStream_t stream) {
  Params p;
  p.x = (const u16*)d_in[0]; p.ckv = (const u16*)d_in[1]; p.wq = (const u16*)d_in[2]; p.wuk = (const u16*)d_in[3];
  p.wuv = (const u16*)d_in[4]; p.wo = (const u16*)d_in[5]; p.w1 = (const u16*)d_in[7]; p.b1 = (const u16*)d_in[8];
  p.w2 = (const u16*)d_in[9]; p.b2 = (const u16*)d_in[10]; p.c = (const u16*)d_in[11]; p.wmod = (const u16*)d_in[12];
  p.out = (u16*)d_out; p.ws = (unsigned char*)d_ws;
  (void)hipFuncSetAttribute((const void*)fused_decode, hipFuncAttributeMaxDynamicSharedMemorySize, LDS_BYTES);
  hipLaunchKernelGGL(fused_decode, dim3(256), dim3(512), LDS_BYTES, stream, p);
}
```

```cpp
#include <hip/hip_runtime.h>
#include <hip/hip_bf16.h>
#include <stdint.h>

#define LAS __attribute__((address_space(3)))
#define GAS __attribute__((address_space(1)))
typedef unsigned short u16;
typedef unsigned v4u __attribute__((ext_vector_type(4)));
typedef unsigned v2u __attribute__((ext_vector_type(2)));
typedef float f4 __attribute__((ext_vector_type(4)));
typedef float f2 __attribute__((ext_vector_type(2)));
typedef short s8v __attribute__((ext_vector_type(8)));
typedef short s4v __attribute__((ext_vector_type(4)));
typedef __bf16 bf2v __attribute__((ext_vector_type(2)));
typedef int i4v __attribute__((ext_vector_type(4)));
typedef unsigned short us8v __attribute__((ext_vector_type(8)));
typedef __amdgpu_buffer_rsrc_t rsrc_t;

constexpr int SK = 4096, NM = 640, DC = 512, FF = 1728, HD = 4096;

#define MODPF 1
#define W1MODE 0
#define INFL1 6
#define NSLOW 4
#define N37 8
#define DLY_CLR 8
#define INFL 6
#define UKPF 3
#define DLY_PF 1
#define N3 4
#define DLY_HELP 28
#define CODE_LEN 20480
#define DLY_WQ 3
#define DLY_UK 8
#define DLY_CKV 4

constexpr int WS_E = 0;
constexpr int WS_PR = 256;
constexpr int WS_CF = 4096;
constexpr int WS_H1 = 16384;
constexpr int WS_H2 = 180224;
constexpr int WS_H3 = 442368;
constexpr int WS_ST = 507904;
constexpr int WS_H5 = 524288;
constexpr int WS_H6 = 589824;
constexpr int WS_H7 = 655360;
constexpr int WS_MV = 671744;
constexpr int WS_CLR = WS_H3;
constexpr int WS_H4 = 2097152;
constexpr int WS_BYTES = 16 << 20;
constexpr int STAMP_OFF = 8 << 20;
constexpr unsigned SENT = 0xFFFFFFFFu;
constexpr unsigned SPIN_CAP = 30000u;

constexpr int L_CKV = 0, L_QS = 131072, L_AM = 147456;
constexpr int L_W2 = 81920;
constexpr int L_MX = L_AM, L_LS = L_AM + 256, L_PB = L_AM + 512, L_ARED = L_AM + 4608, L_ASC = L_AM + 8704;
constexpr int L_WQ = 0, L_WUV = 0, L_UK = 40960, L_ROLE = 40960, L_CM = 122880;
constexpr int L_HS = L_CM, L_QH = L_CM + 640, L_SC = L_CM + 1280, L_CRED = L_CM + 3840, L_GS = L_CM + 8192, L_MU = L_CM + 1152;
constexpr int LDS_BYTES = 163840, L_SCR = LDS_BYTES - 512, L_FLIP = LDS_BYTES - 528;

#define STAMP(i) do {} while (0)
#define STAMPW(i, w) do {} while (0)
#define ST_ARG
#define ST_PASS

struct Params {
  const u16 *x, *ckv, *wq, *wuk, *wuv, *wo, *w1, *b1, *w2, *b2, *c, *wmod;
  u16* out;
  unsigned char* ws;
};

__device__ __forceinline__ float blo(unsigned w) { return __uint_as_float(w << 16); }
__device__ __forceinline__ float bhi(unsigned w) { return __uint_as_float(w & 0xffff0000u); }
__device__ __forceinline__ unsigned pk(float lo, float hi) { f2 f = {lo, hi}; return __builtin_bit_cast(unsigned, __builtin_convertvector(f, bf2v)); }
__device__ __forceinline__ float rbf(float f) { return blo(pk(f, 0.f)); }
__device__ __forceinline__ unsigned nosent(unsigned v) { return v < 0xFFFFFFFEu ? v : 0xFFFFFFFEu; }
__device__ __forceinline__ f4 mfma(v4u a, v4u b, f4 c) { return __builtin_amdgcn_mfma_f32_16x16x32_bf16(__builtin_bit_cast(s8v, a), __builtin_bit_cast(s8v, b), c, 0, 0, 0); }
template <int C> __device__ __forceinline__ float dpp(float x) { return __builtin_bit_cast(float, __builtin_amdgcn_mov_dpp(__builtin_bit_cast(int, x), C, 0xf, 0xf, true)); }
template <int C> __device__ __forceinline__ unsigned dppu(unsigned x) { return (unsigned)__builtin_amdgcn_mov_dpp((int)x, C, 0xf, 0xf, true); }
__device__ __forceinline__ float sum16(float a, float b) { auto s = __builtin_amdgcn_permlane16_swap(__float_as_uint(a), __float_as_uint(b), false, false); return __uint_as_float(s[0]) + __uint_as_float(s[1]); }
__device__ __forceinline__ float sum32(float a, float b) { auto s = __builtin_amdgcn_permlane32_swap(__float_as_uint(a), __float_as_uint(b), false, false); return __uint_as_float(s[0]) + __uint_as_float(s[1]); }
__device__ __forceinline__ float vmax(float a, float b) { return __builtin_elementwise_maximum(a, b); }
__device__ __forceinline__ float vmax3(float a, float b, float c) { return vmax(vmax(a, b), c); }
__device__ __forceinline__ float max16(float a) { auto s = __builtin_amdgcn_permlane16_swap(__float_as_uint(a), __float_as_uint(a), false, false); return vmax(__uint_as_float(s[0]), __uint_as_float(s[1])); }
__device__ __forceinline__ float max32(float a) { auto s = __builtin_amdgcn_permlane32_swap(__float_as_uint(a), __float_as_uint(a), false, false); return vmax(__uint_as_float(s[0]), __uint_as_float(s[1])); }
__device__ __forceinline__ float rsum32(float v) {
  v += dpp<0xB1>(v); v += dpp<0x4E>(v); v += dpp<0x141>(v); v += dpp<0x140>(v); return sum16(v, v);
}
__device__ __forceinline__ float rdl(float v, int l) { return __builtin_bit_cast(float, __builtin_amdgcn_readlane(__builtin_bit_cast(int, v), l)); }
__device__ __forceinline__ float dot2(unsigned a, unsigned b, float c) { return __builtin_amdgcn_fdot2_f32_bf16(__builtin_bit_cast(bf2v, a), __builtin_bit_cast(bf2v, b), c, false); }

__device__ __forceinline__ rsrc_t mkrs(const void* p, int bytes) { return __builtin_amdgcn_make_buffer_rsrc((void*)p, 0, bytes, 0x00020000); }
__device__ __forceinline__ v4u ld16(rsrc_t r, int off) { return __builtin_amdgcn_raw_buffer_load_b128(r, off, 0, 16); }
__device__ __forceinline__ v2u ld8(rsrc_t r, int off) { return __builtin_amdgcn_raw_buffer_load_b64(r, off, 0, 16); }
__device__ __forceinline__ unsigned ld4(rsrc_t r, int off) { return __builtin_amdgcn_raw_buffer_load_b32(r, off, 0, 16); }
__device__ __forceinline__ void st16(rsrc_t r, int off, v4u v) { __builtin_amdgcn_raw_buffer_store_b128(v, r, off, 0, 16); }
__device__ __forceinline__ void st8(rsrc_t r, int off, v2u v) { __builtin_amdgcn_raw_buffer_store_b64(v, r, off, 0, 16); }
__device__ __forceinline__ void st4(rsrc_t r, int off, unsigned v) { __builtin_amdgcn_raw_buffer_store_b32(v, r, off, 0, 16); }
__device__ __forceinline__ v4u ld16s(rsrc_t r, int off, int so) { return __builtin_amdgcn_raw_buffer_load_b128(r, off, so, 16); }
__device__ __forceinline__ void st16s(rsrc_t r, int off, int so, v4u v) { __builtin_amdgcn_raw_buffer_store_b128(v, r, off, so, 16); }
__device__ __forceinline__ v4u gld16(const void* p) { return *(const GAS v4u*)p; }
__device__ __forceinline__ unsigned ldE(rsrc_t r) { return __builtin_amdgcn_raw_buffer_load_b32(r, WS_E, 0, 0); }
__device__ __forceinline__ void dma(rsrc_t r, int voff, int soff, int lds_dst) {
  asm volatile("s_mov_b32 m0, %0\n\ts_nop 0\n\tbuffer_load_dwordx4 %1, %2, %3 offen nt lds" :: "s"(lds_dst), "v"(voff), "s"(r), "s"(soff) : "m0", "memory");
}
__device__ __forceinline__ void dma_t(rsrc_t r, int voff, int soff, int lds_dst) {
  asm volatile("s_mov_b32 m0, %0\n\ts_nop 0\n\tbuffer_load_dwordx4 %1, %2, %3 offen lds" :: "s"(lds_dst), "v"(voff), "s"(r), "s"(soff) : "m0", "memory");
}
__device__ __forceinline__ void l2pf(rsrc_t r, int voff, int scr) {
  asm volatile("s_mov_b32 m0, %0\n\ts_nop 0\n\tbuffer_load_dword %1, %2, 0 offen lds" :: "s"(scr), "v"(voff), "s"(r) : "m0", "memory");
}
__device__ __forceinline__ void l2pf_nt(rsrc_t r, int voff, int scr) {
  asm volatile("s_mov_b32 m0, %0\n\ts_nop 0\n\tbuffer_load_dword %1, %2, 0 offen nt lds" :: "s"(scr), "v"(voff), "s"(r) : "m0", "memory");
}
#define BAR() asm volatile("s_waitcnt lgkmcnt(0)\n\ts_barrier" ::: "memory")
#define VMW(n) asm volatile("s_waitcnt vmcnt(%0)" :: "n"(n) : "memory")
#define RELOAD() asm volatile("" ::: "memory")
#define PIN(v) asm volatile("" : "+v"(v))
#define SCHED() __builtin_amdgcn_sched_barrier(0)
__device__ __forceinline__ unsigned mx4(v4u v) { return max(max(v.x, v.y), max(v.z, v.w)); }
__device__ __forceinline__ bool nos4(v4u v) { return mx4(v) != SENT; }
__device__ __forceinline__ int fsw(int k) { return (k & 3) ^ ((k & 4) ? 12 : 0) ^ ((k & 8) ? 4 : 0); }

#define LDS_RD128(off) (*(const LAS v4u*)(lds + (off)))
#define LDS_RD64(off) (*(const LAS v2u*)(lds + (off)))
#define LDS_RDF(off) (*(const LAS float*)(lds + (off)))
#define LDS_WR128(off, v) (*(LAS v4u*)(lds + (off)) = (v))
#define LDS_WR64(off, v) (*(LAS v2u*)(lds + (off)) = (v))
#define LDS_WR32(off, v) (*(LAS unsigned*)(lds + (off)) = (v))
#define LDS_WRF(off, v) (*(LAS float*)(lds + (off)) = (v))

template <int RB> __device__ __forceinline__ int swz(int row) {
  if (RB == 256) return 2 * ((row & 3) | (((row >> 3) & 1) << 2));
  if (RB == 128) return 2 * (((row >> 1) & 1) | (((row >> 3) & 1) << 1));
  if (RB == 64) return 2 * ((row >> 3) & 1);
  return 0;
}
__device__ __forceinline__ v2u trrd(LAS unsigned char* lds, int off) { return __builtin_bit_cast(v2u, __builtin_amdgcn_ds_read_tr16_b64_v4i16((LAS s4v*)(lds + off))); }
template <int RB> __device__ __forceinline__ int tr_lane(int ctile, int lane) {
  const int g4 = lane >> 4, q = (lane & 15) >> 2, p = lane & 3, r0 = 8 * g4 + q;
  if (RB == 32) return (r0 ^ ((g4 & 1) << 2)) * 32 + p * 8;
  return r0 * RB + (((2 * ctile + (p >> 1)) ^ swz<RB>(r0)) << 4) + (p & 1) * 8;
}
template <int RB> __device__ __forceinline__ v4u trfrag(LAS unsigned char* lds, int off) {
  v2u a = trrd(lds, off), b = trrd(lds, RB == 32 ? (off ^ 128) : off + 4 * RB);
  return (v4u){a.x, a.y, b.x, b.y};
}
template <int RB> __device__ __forceinline__ int dma_lane(int ld, int lane, int par) {
  constexpr int CPR = RB / 16;
  const int r = lane / CPR, pos = lane % CPR;
  if (RB == 32) return (r ^ (((r >> 3) & 1) << 2)) * ld + pos * 16;
  return r * ld + ((pos ^ swz<RB>(r + 8 * par)) << 4);
}
__device__ __forceinline__ v4u silu8(v4u c) {
  v4u o;
#pragma unroll
  for (int i = 0; i < 4; ++i) { float a = blo(c[i]), b = bhi(c[i]); o[i] = pk(a * __builtin_amdgcn_rcpf(1.f + __expf(-a)), b * __builtin_amdgcn_rcpf(1.f + __expf(-b))); }
  return o;
}
__device__ __forceinline__ v4u quad_gather(unsigned v) { return (v4u){dppu<0x00>(v), dppu<0x55>(v), dppu<0xAA>(v), dppu<0xFF>(v)}; }

__device__ __forceinline__ unsigned poll_votes(rsrc_t ws, unsigned tag, int lane) {
  const int off = WS_CF + (lane >> 5) * 8192 + min(lane & 31, 27) * 128;
  unsigned long long ones = 0;
  for (unsigned sp = 0; sp < SPIN_CAP; ++sp) {
    RELOAD();
    const v4u v = ld16(ws, off);
    const unsigned long long ok = __ballot(v.x == tag), ok2 = __ballot(v.z == tag);
    ones = __ballot((v.x == tag) & (v.y == 1u));
    if ((((unsigned)ok | (unsigned)(ok >> 32)) & ((unsigned)ok2 | (unsigned)(ok2 >> 32))) == 0xFFFFFFFFu) break;
  }
  return __builtin_popcount(((unsigned)ones | (unsigned)(ones >> 32)) & 0x0FFFFFFFu) >= 14 ? 1u : 0u;
}

#define UK_PREFETCH() do { _Pragma("unroll 1") for (int n_ = 0; n_ < 8; ++n_) dma(mkrs(p.wuk, DC * HD * 2), lane * 16, ((g >> 3) * 32 + (w - 4) * 8 + n_) * HD * 2 + (g & 7) * 1024, L_ARED); } while (0)
#define UK_N 8
__device__ __forceinline__ void a_path(const Params& p, LAS unsigned char* lds, rsrc_t ws, int g, int w, int lane, unsigned tag ST_ARG) {
  const int t = threadIdx.x, xcd = g & 7, r = g >> 3, b = xcd >> 2, s = (xcd & 3) * 8 + (r >> 1), hg = r & 1;
  const int i = lane & 15, g4 = lane >> 4;
  const bool s0 = g < 100;
  const rsrc_t rk = mkrs(p.ckv + ((size_t)b * SK + s * 128) * DC, 131072);
  const int l16 = lane << 4, rot = hg * 64;
#define CKV_ROW(k_) do { const int kk_ = (k_); dma(rk, l16 ^ (fsw(kk_ & 15) << 4), ((kk_ + rot) & 127) * 1024, L_CKV + kk_ * 1024); } while (0)
  if (w >= 4) { __builtin_amdgcn_s_sleep(DLY_PF); UK_PREFETCH(); }
  __builtin_amdgcn_s_sleep(DLY_CKV);
#pragma unroll 1
  for (int n = 0; n < NSLOW; ++n) { VMW(INFL1 - 1); CKV_ROW(n * 8 + w); }
#pragma unroll 1
  for (int n = NSLOW; n < 16; ++n) { VMW(INFL - 1); CKV_ROW(n * 8 + w); }
  if (w == 0) { const unsigned f = poll_votes(ws, tag, lane); if (lane == 0) LDS_WR32(L_FLIP, f); }
  VMW(INFL < 8 ? INFL : 8);
  BAR();
  STAMP(3);
  const int fl = __builtin_amdgcn_readfirstlane(*(const LAS unsigned*)(lds + L_FLIP)), myc = (b ^ fl) * 8192;
  int h4o = WS_H4 + ((4 * g4 * 4 + (w & 3)) * 2 + b) * 16384 + (hg ^ fl ^ 1) * 8192 + s * 256 + i * 16;
  {
    const v4u sv = {SENT, SENT, SENT, SENT};
    st16(ws, h4o + (w >> 2) * 262144, sv); st16(ws, h4o + (w >> 2) * 262144 + 131072, sv);
  }
  v4u fr[32];
  int qa[4];
#pragma unroll
  for (int k4 = 0; k4 < 4; ++k4) qa[k4] = i * 1024 + ((((4 * k4 + g4) ^ fsw(i))) << 4);
#pragma unroll
  for (int hf = 0; hf < 2; ++hf) {
    if (hf == 1) {
      VMW(2);
      BAR();
      STAMP(4);
    }
    if (w < 4) {
#pragma unroll
      for (int ks = 0; ks < 16; ++ks) fr[hf * 16 + ks] = LDS_RD128(L_CKV + (64 * hf + 16 * w) * 1024 + qa[ks & 3] + (ks >> 2) * 256);
    } else {
      const int pw = w - 4;
#pragma unroll
      for (int u = 0; u < 4; ++u) {
        v4u X[4];
#pragma unroll
        for (int jj = 0; jj < 4; ++jj) { const int kl = 4 * g4 + jj; X[jj] = LDS_RD128(L_CKV + (64 * hf + 16 * u + kl) * 1024 + pw * 256 + ((i ^ fsw(kl)) << 4)); }
#pragma unroll
        for (int tt = 0; tt < 8; ++tt)
#pragma unroll
          for (int m = 0; m < 2; ++m) fr[u * 8 + tt][2 * hf + m] = __builtin_amdgcn_perm(X[2 * m + 1][tt >> 1], X[2 * m][tt >> 1], (tt & 1) ? 0x07060302u : 0x05040100u);
      }
    }
    if (hf == 0) {
#pragma unroll
      for (int n = 0; n < 32; ++n) PIN(fr[n]);
    }
  }
#pragma unroll
  for (int n = 0; n < 32; ++n) PIN(fr[n]);
#pragma unroll
  for (int k4 = 0; k4 < 4; ++k4) { qa[k4] += L_QS; PIN(qa[k4]); }
  const bool r5 = r < 10, r7 = (r >= 10) & (r < 15);
  const int mt7 = xcd * 5 + (r - 10);
  const rsrc_t rr = r5 ? mkrs(p.wo, HD * NM * 2) : mkrs(p.wmod, NM * 3840 * 2);
  const int rld = r5 ? NM * 2 : 7680;
  int rl0 = dma_lane<128>(rld, lane, 0), rl1 = dma_lane<128>(rld, lane, 1);
  PIN(rl0); PIN(rl1);
  const int rso = r5 ? (512 * xcd * NM + 64 * r) * 2 : (1280 + 64 * mt7) * 2;
#define ROLE_INS(n_) do { const int in_ = (n_); dma(rr, (in_ & 1) ? rl1 : rl0, rso + in_ * 8 * rld, L_CKV + in_ * 1024); } while (0)
  int mxw = L_MX + i * 16 + (w & 3) * 4, mxr = L_MX + i * 16, pbw = L_PB + (w & 3) * 1024 + l16, pbr = L_PB + l16;
  PIN(h4o); PIN(mxw); PIN(mxr); PIN(pbw); PIN(pbr);
  STAMP(5);
  {
    const int o0 = WS_H3 + (hg * 2) * 16384 + myc + t * 16;
    int d0 = L_QS + w * 1024 + ((lane ^ fsw(w)) << 4), d1 = L_QS + (w + 8) * 1024 + ((lane ^ fsw(w + 8)) << 4);
    PIN(d0); PIN(d1);
    v4u q0, q1;
    for (unsigned sp = 0; sp < SPIN_CAP; ++sp) { RELOAD(); q0 = ld16(ws, o0); q1 = ld16(ws, o0 + 16384); if (__all(max(mx4(q0), mx4(q1)) != SENT)) break; }
    LDS_WR128(d0, q0); LDS_WR128(d1, q1);
  }
#define RESCALE(u_) do { const unsigned dd_ = min((unsigned)(mrow - m4[u_]), 255u) * 0x00800080u; \
    pf[u_] = __builtin_bit_cast(v4u, __builtin_elementwise_sub_sat(__builtin_bit_cast(us8v, pf[u_]), __builtin_bit_cast(us8v, (v4u){dd_, dd_, dd_, dd_}))); } while (0)
  if (w < 4) {
    BAR();
    STAMP(6);
    f4 sacc[2];
    sacc[0] = (f4){0.f, 0.f, 0.f, 0.f}; sacc[1] = sacc[0];
    v4u qf[16];
#pragma unroll
    for (int ks = 0; ks < 4; ++ks) qf[ks] = LDS_RD128(qa[ks]);
    SCHED();
#pragma unroll
    for (int ks = 4; ks < 12; ++ks) qf[ks] = LDS_RD128(qa[ks & 3] + (ks >> 2) * 256);
    SCHED();
#pragma unroll
    for (int ks = 0; ks < 4; ++ks) { sacc[0] = mfma(fr[ks], qf[ks], sacc[0]); sacc[1] = mfma(fr[16 + ks], qf[ks], sacc[1]); qf[12 + ks] = LDS_RD128(qa[ks] + 768); SCHED(); }
#pragma unroll
    for (int ks = 4; ks < 16; ++ks) { sacc[0] = mfma(fr[ks], qf[ks], sacc[0]); sacc[1] = mfma(fr[16 + ks], qf[ks], sacc[1]); }
    float mx = vmax3(vmax3(sacc[0][0], sacc[0][1], sacc[0][2]), vmax3(sacc[0][3], sacc[1][0], sacc[1][1]), vmax(sacc[1][2], sacc[1][3]));
    mx = __builtin_ceilf(max32(max16(mx)));
    float pp[8];
#pragma unroll
    for (int n = 0; n < 8; ++n) pp[n] = __builtin_amdgcn_exp2f(sacc[n >> 2][n & 3] - mx);
    LDS_WR32(mxw, (unsigned)(int)mx);
    LDS_WR128(pbw, ((v4u){pk(pp[0], pp[1]), pk(pp[2], pp[3]), pk(pp[4], pp[5]), pk(pp[6], pp[7])}));
    STAMP(7);
    BAR();
    if (w == 0) {
      const i4v m4 = __builtin_bit_cast(i4v, LDS_RD128(mxr));
      v4u pf[4];
#pragma unroll
      for (int u = 0; u < 4; ++u) pf[u] = LDS_RD128(pbr + u * 1024);
      const int mrow = max(max(m4[0], m4[1]), max(m4[2], m4[3]));
      RESCALE(0); RESCALE(1); RESCALE(2); RESCALE(3);
      const v4u ones = {0x3F803F80u, 0x3F803F80u, 0x3F803F80u, 0x3F803F80u};
      f4 l4 = {0.f, 0.f, 0.f, 0.f};
#pragma unroll
      for (int u = 0; u < 4; ++u) l4 = mfma(ones, pf[u], l4);
      if (lane < 16) st8(ws, WS_ST + (hg ^ fl) * 8192 + (b * 32 + s) * 128 + lane * 8, (v2u){nosent(__float_as_uint((float)mrow)), nosent(__float_as_uint(l4[0]))});
      STAMP(8);
    } else if (r7) {
      v4u cc;
      asm volatile("buffer_load_dwordx4 %0, %1, %2, 0 offen\n\ts_waitcnt vmcnt(0)" : "=&v"(cc) : "v"((t - 64) * 16), "s"(mkrs(p.c, 2560)) : "memory");
      LDS_WR128(L_ASC + (t - 64) * 16, silu8(cc));
    }
    BAR();
  } else {
    BAR();
    if (r5 | r7) {
      const int n3 = r5 ? 4 * N3 : 4 * N37;
#pragma unroll 1
      for (int n = w - 4; n < n3; n += 4) ROLE_INS(n);
    }
    int h4a = h4o + 131072, h4b = h4o + 262144, h4c = h4o + 393216;
    PIN(h4a); PIN(h4b); PIN(h4c);
    BAR();
    const i4v m4 = __builtin_bit_cast(i4v, LDS_RD128(mxr));
    v4u pf[4];
#pragma unroll
    for (int u = 0; u < 4; ++u) pf[u] = LDS_RD128(pbr + u * 1024);
    SCHED();
    const int mrow = max(max(m4[0], m4[1]), max(m4[2], m4[3]));
    f4 o[8];
    v4u ov[4];
    const f4 zz = {0.f, 0.f, 0.f, 0.f};
    RESCALE(0); SCHED();
    o[0] = mfma(pf[0], fr[0], zz); o[1] = mfma(pf[0], fr[1], zz); RESCALE(1); o[2] = mfma(pf[0], fr[2], zz); o[3] = mfma(pf[0], fr[3], zz); SCHED();
    o[0] = mfma(pf[1], fr[8], o[0]); o[1] = mfma(pf[1], fr[9], o[1]); RESCALE(2); o[2] = mfma(pf[1], fr[10], o[2]); o[3] = mfma(pf[1], fr[11], o[3]); SCHED();
    o[0] = mfma(pf[2], fr[16], o[0]); o[1] = mfma(pf[2], fr[17], o[1]); RESCALE(3); o[2] = mfma(pf[2], fr[18], o[2]); o[3] = mfma(pf[2], fr[19], o[3]); SCHED();
    o[0] = mfma(pf[3], fr[24], o[0]); o[1] = mfma(pf[3], fr[25], o[1]); o[2] = mfma(pf[3], fr[26], o[2]); o[3] = mfma(pf[3], fr[27], o[3]); SCHED();
#pragma unroll
    for (int u = 0; u < 4; ++u) { o[4] = mfma(pf[u], fr[u * 8 + 4], u ? o[4] : zz); o[5] = mfma(pf[u], fr[u * 8 + 5], u ? o[5] : zz); if (u == 1) SCHED(); if (u >= 2) { ov[2 * u - 4][0] = nosent(pk(o[0][2 * u - 4], o[1][2 * u - 4])); ov[2 * u - 3][0] = nosent(pk(o[0][2 * u - 3], o[1][2 * u - 3])); } }
    SCHED();
#pragma unroll
    for (int u = 0; u < 4; ++u) {
      o[6] = mfma(pf[u], fr[u * 8 + 6], u ? o[6] : zz); o[7] = mfma(pf[u], fr[u * 8 + 7], u ? o[7] : zz);
      if (u < 2) { ov[2 * u][1] = nosent(pk(o[2][2 * u], o[3][2 * u])); ov[2 * u + 1][1] = nosent(pk(o[2][2 * u + 1], o[3][2 * u + 1])); }
      else { ov[2 * u - 4][2] = nosent(pk(o[4][2 * u - 4], o[5][2 * u - 4])); ov[2 * u - 3][2] = nosent(pk(o[4][2 * u - 3], o[5][2 * u - 3])); }
      SCHED();
    }
    ov[0][3] = nosent(pk(o[6][0], o[7][0])); st16(ws, h4o, ov[0]);
    ov[1][3] = nosent(pk(o[6][1], o[7][1])); st16(ws, h4a, ov[1]);
    ov[2][3] = nosent(pk(o[6][2], o[7][2])); st16(ws, h4b, ov[2]);
    ov[3][3] = nosent(pk(o[6][3], o[7][3])); st16(ws, h4c, ov[3]);
    STAMPW(9, 4);
    VMW(0);
    STAMPW(20, 4);
    BAR();
  }
  if (r5) {
#pragma unroll 1
    for (int n = 4 * N3 + w; n < 64; n += 8) ROLE_INS(n);
    VMW(0);
    BAR();
    STAMP(10);
    v4u wf[8];
#pragma unroll
    for (int mt = 0; mt < 4; ++mt) {
      const int a = L_CKV + 64 * w * 128 + tr_lane<128>(mt, lane);
      wf[mt] = trfrag<128>(lds, a); wf[4 + mt] = trfrag<128>(lds, a + 4096);
    }
#pragma unroll
    for (int n = 0; n < 8; ++n) PIN(wf[n]);
    v4u a0 = {0u, 0u, 0u, 0u}, a1 = a0;
    const int hsrc = 4 * xcd + (w >> 1);
    const int o5 = WS_H5 + (i >> 1) * 16384 + myc + (i & 1) * 4096 + (hsrc & 15) * 256 + (w & 1) * 128 + g4 * 16;
    int rw = L_ARED + ((w * 2 + g4) * 64 + i) * 4, rd = L_ARED + ((t >> 5) * 64 + 2 * (t & 31)) * 4;
    int o6 = WS_H6 + (xcd >> 1) * 16384 + (xcd & 1) * 4096 + ((t >> 5) & 1) * 1280 + r * 128 + (t & 28) * 4 + (t & 1) * 8192;
    PIN(rw); PIN(rd); PIN(o6);
    for (unsigned sp = 0; sp < SPIN_CAP; ++sp) { RELOAD(); if (i < 8) { a0 = ld16(ws, o5); a1 = ld16(ws, o5 + 64); } if (__all(max(mx4(a0), mx4(a1)) != SENT)) break; }
    STAMP(11);
    f4 d[4];
#pragma unroll
    for (int mt = 0; mt < 4; ++mt) d[mt] = mfma(a0, wf[mt], (f4){0.f, 0.f, 0.f, 0.f});
#pragma unroll
    for (int mt = 0; mt < 4; ++mt) d[mt] = mfma(a1, wf[4 + mt], d[mt]);
#pragma unroll
    for (int mt = 0; mt < 4; ++mt) {
      const float v = sum16(d[mt][0] + d[mt][2], d[mt][1] + d[mt][3]);
      if (lane < 32) LDS_WRF(rw + 64 * mt, v);
    }
    BAR();
    if (w == 0) {
      v2u v[8];
#pragma unroll
      for (int ww = 0; ww < 8; ++ww) v[ww] = LDS_RD64(rd + ww * 512);
      const float s0_ = ((__uint_as_float(v[0].x) + __uint_as_float(v[1].x)) + (__uint_as_float(v[2].x) + __uint_as_float(v[3].x))) + ((__uint_as_float(v[4].x) + __uint_as_float(v[5].x)) + (__uint_as_float(v[6].x) + __uint_as_float(v[7].x)));
      const float s1_ = ((__uint_as_float(v[0].y) + __uint_as_float(v[1].y)) + (__uint_as_float(v[2].y) + __uint_as_float(v[3].y))) + ((__uint_as_float(v[4].y) + __uint_as_float(v[5].y)) + (__uint_as_float(v[6].y) + __uint_as_float(v[7].y)));
      const v4u o = quad_gather(nosent(pk(s0_, s1_)));
      if ((lane & 3) < 2) st16(ws, o6, o);
    }
    STAMP(12);
  } else if (r7) {
#pragma unroll 1
    for (int n = 4 * N37 + w; n < 80; n += 8) ROLE_INS(n);
    VMW(0);
    BAR();
    STAMP(19);
    {
      f4 d = {0.f, 0.f, 0.f, 0.f};
      const int a = L_CKV + (w >> 2) * 40960 + tr_lane<128>(w & 3, lane), sa = L_ASC + (i & 1) * 1280 + (w >> 2) * 640 + g4 * 16;
      v4u af[10], bf[10];
#pragma unroll
      for (int ks = 0; ks < 10; ++ks) { af[ks] = LDS_RD128(sa + ks * 64); bf[ks] = trfrag<128>(lds, a + ks * 4096); }
      f4 d2 = d;
#pragma unroll
      for (int ks = 0; ks < 5; ++ks) { d = mfma(af[2 * ks], bf[2 * ks], d); d2 = mfma(af[2 * ks + 1], bf[2 * ks + 1], d2); }
      d += d2;
      if (w >= 4 && lane < 16) LDS_WR64(L_ARED + ((w - 4) * 16 + lane) * 8, ((v2u){__float_as_uint(d[0]), __float_as_uint(d[1])}));
      BAR();
      if (w < 4) {
        const v2u o = LDS_RD64(L_ARED + (w * 16 + i) * 8);
        const int n = 64 * mt7 + 16 * w + i;
        if (lane < 32) st4(ws, WS_MV + (n >= 1920 ? 16384 + (n - 1920) * 4 : n * 4) + g4 * 8192, nosent(pk(d[0] + __uint_as_float(o.x), d[1] + __uint_as_float(o.y))));
        VMW(0);
      }
      STAMP(11);
      BAR();
    }
    {
      const rsrc_t r2 = mkrs(p.w2, FF * NM * 2);
      const int l2 = dma_lane<32>(NM * 2, lane, 0), rot2 = (mt7 & 3) * 14;
#pragma unroll 1
      for (int n = w; n < 54; n += 8) { int in = n + rot2; if (in >= 54) in -= 54; dma(r2, l2, 32 * mt7 + in * 32 * NM * 2, L_W2 + in * 1024); }
    }
    VMW(0);
    BAR();
    STAMP(10);
    v4u wf[7];
    {
      const int a = L_W2 + w * 1024 + tr_lane<32>(0, lane);
#pragma unroll
      for (int n = 0; n < 6; ++n) wf[n] = trfrag<32>(lds, a + n * 8192);
      wf[6] = trfrag<32>(lds, a + (w < 6 ? 6 * 8192 : 0));
    }
#pragma unroll
    for (int n = 0; n < 7; ++n) PIN(wf[n]);
    float x1v = 0.f, gbv = 0.f, b2v = 0.f;
    if (w == 0) {
      const int bb = g4 & 1, m = 16 * mt7 + i;
      const float xv = blo(p.x[bb * NM + m]); b2v = blo(p.b2[m]);
      unsigned ga, gb, pa[8];
      for (unsigned sp = 0; sp < SPIN_CAP; ++sp) { RELOAD(); ga = ld4(ws, WS_MV + myc + m * 4); gb = ld4(ws, WS_MV + 16384 + myc + m * 4); if (__all((ga != SENT) & (gb != SENT))) break; }
      for (unsigned sp = 0; sp < SPIN_CAP; ++sp) {
        RELOAD();
        bool ok = true;
#pragma unroll
        for (int k = 0; k < 8; ++k) pa[k] = ld4(ws, WS_H6 + (k >> 1) * 16384 + myc + (k & 1) * 4096 + bb * 1280 + (m >> 1) * 4);
#pragma unroll
        for (int k = 0; k < 8; ++k) ok &= pa[k] != SENT;
        if (__all(ok)) break;
      }
      float a = 0.f;
#pragma unroll
      for (int k = 0; k < 8; ++k) a += (m & 1) ? bhi(pa[k]) : blo(pa[k]);
      x1v = rbf(xv + rbf((bb ? bhi(ga) : blo(ga)) * rbf(a)));
      gbv = bb ? bhi(gb) : blo(gb);
      PIN(x1v); PIN(gbv); PIN(b2v);
    }
    STAMP(12);
    v4u af[7];
#pragma unroll
    for (int n = 0; n < 7; ++n) af[n] = (v4u){0u, 0u, 0u, 0u};
    const int o7 = WS_H7 + myc + w * 128 + (i & 1) * 64 + g4 * 16, o76 = o7 + (w < 6 ? 6 * 1024 : 0);
    for (unsigned sp = 0; sp < SPIN_CAP; ++sp) {
      RELOAD();
      if (i < 2) {
#pragma unroll
        for (int n = 0; n < 6; ++n) af[n] = ld16(ws, o7 + n * 1024);
        af[6] = ld16(ws, o76);
      }
      unsigned mm = 0;
#pragma unroll
      for (int n = 0; n < 7; ++n) mm = max(mm, mx4(af[n]));
      if (__all(mm != SENT)) break;
    }
    STAMP(13);
    f4 d = {0.f, 0.f, 0.f, 0.f}, d2 = d;
#pragma unroll
    for (int n = 0; n < 3; ++n) { d = mfma(af[2 * n], wf[2 * n], d); d2 = mfma(af[2 * n + 1], wf[2 * n + 1], d2); }
    if (w < 6) d = mfma(af[6], wf[6], d);
    if (lane < 16) { LDS_WRF(L_ARED + (w * 32 + lane) * 4, d[0] + d2[0]); LDS_WRF(L_ARED + (w * 32 + 16 + lane) * 4, d[1] + d2[1]); }
    BAR();
    if (t < 32) {
      float v[8];
#pragma unroll
      for (int ww = 0; ww < 8; ++ww) v[ww] = LDS_RDF(L_ARED + (ww * 32 + t) * 4);
      const float a = ((v[0] + v[1]) + (v[2] + v[3])) + ((v[4] + v[5]) + (v[6] + v[7]));
      const float y = rbf(rbf(a) + b2v);
      p.out[(t >> 4) * NM + 16 * mt7 + (t & 15)] = (u16)pk(x1v + rbf(gbv * y), 0.f);
      if (mt7 == 0 && t == 0) st4(ws, WS_E, tag);
    }
  }
}

__device__ __forceinline__ void c_path(const Params& p, LAS unsigned char* lds, rsrc_t ws, int g, int w, int lane ST_ARG) {
  const int t = threadIdx.x, xcd = g & 7, rc = (g >> 3) - 16, h = xcd * 4 + (rc >> 2), j = rc & 3, iod = xcd >> 2;
  const int i = lane & 15, g4 = lane >> 4;
  const int p6 = xcd + 8 * (rc >> 1), t6 = 2 * p6 + (rc & 1);
  const bool r6 = p6 < 27;
  const unsigned tag = ldE(ws) + 1u;
  float ln0 = 0.f, ln1 = 0.f;
  v4u xa, xb, xc; unsigned xm = 0;
  const int dl0 = dma_lane<256>(HD * 2, lane, 0), dl1 = dma_lane<256>(HD * 2, lane, 1);
  if (w < 3) {
    const u16* xr = p.x + (lane >> 5) * NM + (lane & 31) * 8;
    xa = gld16(xr); xb = gld16(xr + 256); xc = gld16(xr + ((lane & 31) < 16 ? 512 : 0));
    xm = *(const GAS unsigned*)(p.x + (t & 1) * NM + 160 * j + min(t >> 1, 79) * 2);
  } else {
    const rsrc_t rq = mkrs(p.wq, NM * HD * 2);
    const int so = (160 * j * HD + h * 128) * 2 + (w - 3) * 32 * HD * 2;
    __builtin_amdgcn_s_sleep(DLY_WQ);
#pragma unroll
    for (int n = 0; n < 8; ++n) dma(rq, (n & 2) ? dl1 : dl0, so + n * 4 * HD * 2, L_WQ + (w - 3) * 8192 + n * 1024);
  }
  STAMP(1);
  if (w < 3) {
    const bool v3 = (lane & 31) < 16;
    float sm = 0.f;
#pragma unroll
    for (int k = 0; k < 4; ++k) sm += (blo(xa[k]) + bhi(xa[k])) + (blo(xb[k]) + bhi(xb[k])) + (v3 ? blo(xc[k]) + bhi(xc[k]) : 0.f);
    const float mean = rsum32(sm) * (1.f / NM);
    float sq = 0.f;
#pragma unroll
    for (int k = 0; k < 4; ++k) {
      float d;
      d = blo(xa[k]) - mean; sq += d * d; d = bhi(xa[k]) - mean; sq += d * d; d = blo(xb[k]) - mean; sq += d * d; d = bhi(xb[k]) - mean; sq += d * d;
      d = blo(xc[k]) - mean; sq += v3 ? d * d : 0.f; d = bhi(xc[k]) - mean; sq += v3 ? d * d : 0.f;
    }
    const float var = rsum32(sq) * (1.f / NM);
    const float mb = rbf(mean), sd = rbf(sqrtf(rbf(rbf(var) + 1e-5f)));
    const float mb0 = rdl(mb, 0), mb1 = rdl(mb, 32), sd0 = rdl(sd, 0), sd1 = rdl(sd, 32);
    const float m_ = (t & 1) ? mb1 : mb0, s_ = (t & 1) ? sd1 : sd0;
    ln0 = rbf(rbf(blo(xm) - m_) / s_); ln1 = rbf(rbf(bhi(xm) - m_) / s_);
    PIN(ln0); PIN(ln1);
    if ((t & 223) == 0) LDS_WRF(L_MU + (t >> 5) * 4, mean);
  }
  if (w >= 3) VMW(0);
  BAR();
  STAMP(2);
  v4u wf[5];
  {
    const int a = L_WQ + tr_lane<256>(w, lane);
#pragma unroll
    for (int ks = 0; ks < 5; ++ks) wf[ks] = trfrag<256>(lds, a + ks * 8192);
  }
#pragma unroll
  for (int ks = 0; ks < 5; ++ks) PIN(wf[ks]);
  const int h2b = WS_H2 + (h & 15) * 16384;
  int hsr = L_HS + (i & 1) * 320 + g4 * 16, h2o = h2b + (j * 8 + w) * 256 + (i * 4 + g4) * 16, h2i = h2b + (g4 * 8 + w) * 256 + i * 16;
  int qhw = L_QH + ((lane >> 2) & 1) * 256 + (16 * w + 64 * (lane >> 5) + 4 * (lane & 3) + 2 * ((lane >> 3) & 1) + (g4 & 1)) * 2, qhr = L_QH + (i & 1) * 256 + g4 * 16;
  int h3o = WS_H3 + (h >> 3) * 16384 + (i & 1) * 8192 + (h & 7) * 1024 + (128 * j + 32 * w + 8 * g4) * 2;
  PIN(hsr); PIN(h2o); PIN(h2i); PIN(qhw); PIN(qhr); PIN(h3o);
  unsigned one_lo = 0x00003F80u, one_hi = 0x3F800000u;
  asm volatile("" : "+s"(one_lo), "+s"(one_hi));
  if (w < 3) {
    const int wh = t & 1, o1 = WS_H1 + (wh * NM + 160 * j + min(t >> 1, 79) * 2) * 8;
    int hsw = L_HS + wh * 320 + min(t >> 1, 79) * 4; PIN(hsw);
    const unsigned shk = wh ? 0u : 16u, shs = wh ? 16u : 0u;
    float k0, k1, s0, s1;
    for (unsigned sp = 0; sp < SPIN_CAP; ++sp) {
      RELOAD();
      v4u v[5];
      unsigned bad = 0;
#pragma unroll
      for (int k = 0; k < 5; ++k) v[k] = ld16(ws, o1 + k * 10240);
      k0 = k1 = s0 = s1 = 0.f;
#pragma unroll
      for (int k = 0; k < 5; ++k) {
        bad |= (v[k].y ^ tag) | (v[k].w ^ tag);
        s0 += bhi(v[k].x << shs); s1 += bhi(v[k].z << shs); k0 += bhi(v[k].x << shk); k1 += bhi(v[k].z << shk);
      }
      PIN(k0); PIN(k1); PIN(s0); PIN(s1);
      if (__all(bad == 0u)) break;
    }
    STAMP(16);
    const float rc0 = dpp<0xB1>(s0), rc1 = dpp<0xB1>(s1);
    const float sa0 = wh ? rc0 : k0, sa1 = wh ? rc1 : k1, ca0 = wh ? k0 : rc0, ca1 = wh ? k1 : rc1;
    LDS_WR32(hsw, pk(fmaf(ln0, ca0, ln0 + sa0), fmaf(ln1, ca1, ln1 + sa1)));
  }
  if (w == 3) {
    const unsigned f = poll_votes(ws, tag, lane);
    if (lane == 0) LDS_WR32(L_FLIP, f);
    STAMPW(17, 3);
  }
  BAR();
  STAMP(3);
  int fl, myc;
  {
    v4u hf[5];
#pragma unroll
    for (int ks = 0; ks < 5; ++ks) hf[ks] = LDS_RD128(hsr + ks * 64);
    const unsigned flv = *(const LAS unsigned*)(lds + L_FLIP);
    SCHED();
    f4 d = {0.f, 0.f, 0.f, 0.f}, d2 = d;
    d = mfma(wf[0], hf[0], d); d2 = mfma(wf[1], hf[1], d2); d = mfma(wf[2], hf[2], d); d2 = mfma(wf[3], hf[3], d2); d = mfma(wf[4], hf[4], d);
    SCHED();
    fl = __builtin_amdgcn_readfirstlane(flv); myc = (iod ^ fl) * 8192;
    d += d2;
    if (i < 2) {
      st16s(ws, h2o, myc, (v4u){__float_as_uint(d[0]), tag, __float_as_uint(d[1]), tag});
      st16s(ws, h2o + 128, myc, (v4u){__float_as_uint(d[2]), tag, __float_as_uint(d[3]), tag});
    }
  }
  STAMP(4);
  int uka = L_UK + (32 * (w & 3) + 8 * (i >> 2) + (i & 3)) * 256 + ((g4 ^ i) << 4);
  PIN(uka);
  h3o ^= fl << 13;
  if (w >= 4) {
    const rsrc_t ru = mkrs(p.wuk, DC * HD * 2);
    const int so = (128 * j * HD + h * 128) * 2 + (w - 4) * 32 * HD * 2, ul = g4 * HD * 2 + ((i ^ g4) << 4);
#pragma unroll
    for (int n = 0; n < 8; ++n) dma(ru, ul ^ ((n >> 1) << 6), so + n * 4 * HD * 2, L_UK + (w - 4) * 8192 + n * 1024);
    VMW(0);
    STAMPW(18, 4);
  } else {
    v4u v, v2;
    for (unsigned sp = 0; sp < SPIN_CAP; ++sp) { RELOAD(); v = ld16s(ws, h2i, myc); v2 = ld16s(ws, h2i + 1024, myc); if (__all((v.y == tag) & (v.w == tag) & (v2.y == tag) & (v2.w == tag))) break; }
    STAMP(19);
    const float sc = 0.08838834764831845f * 1.4426950408889634f;
    const float q0 = sum16(__uint_as_float(v.x), __uint_as_float(v.z)), q1 = sum16(__uint_as_float(v2.x), __uint_as_float(v2.z));
    *(LAS u16*)(lds + qhw) = (u16)pk(sum32(q0, q1) * sc, 0.f);
  }
  BAR();
  STAMP(5);
  if (w < 4) {
    v4u qf[4], uk[8];
#pragma unroll
    for (int ks = 0; ks < 4; ++ks) { qf[ks] = LDS_RD128(qhr + ks * 64); uk[ks] = LDS_RD128(uka ^ (ks << 6)); uk[4 + ks] = LDS_RD128((uka ^ (ks << 6)) + 1024); }
    SCHED();
    f4 d0 = {0.f, 0.f, 0.f, 0.f}, d1 = d0;
#pragma unroll
    for (int ks = 0; ks < 4; ++ks) { d0 = mfma(uk[ks], qf[ks], d0); d1 = mfma(uk[4 + ks], qf[ks], d1); }
    if (i < 2) st16(ws, h3o, (v4u){nosent(pk(d0[0], d0[1])), nosent(pk(d0[2], d0[3])), nosent(pk(d1[0], d1[1])), nosent(pk(d1[2], d1[3]))});
  }
  STAMP(6);
  BAR();
  {
    const rsrc_t rv = mkrs(p.wuv, DC * HD * 2);
    const int so = (128 * j * HD + h * 128) * 2 + w * 16 * HD * 2;
#pragma unroll
    for (int n = 0; n < 4; ++n) dma(rv, (n & 2) ? dl1 : dl0, so + n * 4 * HD * 2, L_WUV + w * 4096 + n * 1024);
  }
  const rsrc_t r1 = mkrs(p.w1, NM * FF * 2);
  const int w1l = dma_lane<64>(FF * 2, lane, 0);
#define W1_INS(n_) do { int in = (n_) * 8 + w + (t6 & 1) * 20; if (in >= 40) in -= 40; dma(r1, w1l, 64 * t6 + in * 16 * FF * 2, L_ROLE + in * 1024); } while (0)
#define MOD_PF() do { const int u = ((rc - 8) * 8 + w) * 50 + lane; l2pf(mkrs(p.wmod, NM * 3840 * 2), lane < 50 ? (u / 5) * 7680 + 2560 + xcd * 640 + (u % 5) * 128 : 0x7ffffff0, L_SCR); } while (0)
  if (r6 && W1MODE == 0) {
#pragma unroll 1
    for (int n = 0; n < 5; ++n) W1_INS(n);
    VMW(5);
  } else if (rc >= 8 && MODPF == 1) {
    MOD_PF();
    VMW(1);
  } else { VMW(0); }
  BAR();
  STAMP(7);
  if (rc >= 8 && MODPF == 2) MOD_PF();
  if (r6 && W1MODE == 1) {
#pragma unroll 1
    for (int n = 0; n < 5; ++n) W1_INS(n);
  }
  v4u uv[16];
#pragma unroll
  for (int nt = 0; nt < 8; ++nt) {
    const int a = L_WUV + (w & 1) * 16384 + tr_lane<256>(nt, lane);
    uv[nt] = trfrag<256>(lds, a); uv[8 + nt] = trfrag<256>(lds, a + 8192);
  }
#pragma unroll
  for (int n = 0; n < 16; ++n) PIN(uv[n]);
  v4u w1f[5];
  const int tq = min(t, 319), bb6 = tq >= 160, mq = tq - 160 * bb6;
  v2u xx = {0u, 0u}; unsigned b1v = 0;
  if (r6) {
    xx = *(const GAS v2u*)(p.x + bb6 * NM + 4 * mq);
    b1v = *(const GAS unsigned*)(p.b1 + 32 * t6 + 2 * (t & 15));
    VMW(0);
    BAR();
    const int a = L_ROLE + 160 * (w >> 1) * 64 + tr_lane<64>(w & 1, lane);
#pragma unroll
    for (int ks = 0; ks < 5; ++ks) w1f[ks] = trfrag<64>(lds, a + ks * 2048);
#pragma unroll
    for (int ks = 0; ks < 5; ++ks) PIN(w1f[ks]);
    PIN(xx); PIN(b1v);
  }
  STAMP(8);
  {
    const int bb = w >> 2, sh = (w >> 1) & 1, kh = w & 1;
    const int os = WS_ST + myc + (bb * 32 + 16 * (sh ^ ((i >> 2) & 1)) + 4 * g4 + (i & 3)) * 128 + (h & 15) * 8;
    const int oa = WS_H4 + (((h & 15) * 4 + j) * 2 + bb) * 16384 + (myc ^ 8192) + (16 * sh + i) * 256 + kh * 128 + g4 * 16;
    int rw = L_CRED + (w * 128 + 32 * g4 + i) * 4, rd = L_CRED + ((t >> 5) * 512 + 4 * (t & 31)) * 4;
    int o5 = WS_H5 + j * 16384 + myc + ((t >> 5) & 1) * 4096 + (h & 15) * 256 + (t & 31) * 8;
    PIN(rw); PIN(rd); PIN(o5);
    v2u sv = {0u, 0u};
    v4u a0, a1;
    int oa0 = oa, oa1 = oa + 64; PIN(oa0); PIN(oa1);
    for (unsigned sp = 0; sp < SPIN_CAP; ++sp) { RELOAD(); if (i < 8) sv = ld8(ws, os); if (__all(max(sv.x, sv.y) != SENT)) break; }
    STAMP(20);
    a0 = ld16(ws, oa0); a1 = ld16(ws, oa1);
    SCHED();
    const float mv = i < 8 ? __uint_as_float(sv.x) : -__builtin_inff(), lv = i < 8 ? __uint_as_float(sv.y) : 0.f;
    float M = vmax(mv, dpp<0xB1>(mv)); M = vmax(M, dpp<0x4E>(M)); M = vmax(M, dpp<0x141>(M)); M = vmax(M, dpp<0x140>(M)); M = max32(max16(M));
    const float e = __builtin_amdgcn_exp2f(mv - M);
    float L = e * lv; L += dpp<0xB1>(L); L += dpp<0x4E>(L); L += dpp<0x141>(L); L += dpp<0x140>(L); L = sum16(L, L); L = sum32(L, L);
    const float wg = e * __builtin_amdgcn_rcpf(L);
    f2 w01 = {dpp<0x150>(wg), dpp<0x151>(wg)}, w23 = {dpp<0x152>(wg), dpp<0x153>(wg)};
    PIN(w01); PIN(w23);
    for (unsigned sp = 0; sp < SPIN_CAP; ++sp) { if (__all(max(mx4(a0), mx4(a1)) != SENT)) break; RELOAD(); a0 = ld16(ws, oa0); a1 = ld16(ws, oa1); }
    STAMP(9);
    f4 d[8];
    float o[8];
#pragma unroll
    for (int q = 0; q < 4; ++q) {
      d[2 * q] = mfma(a0, uv[2 * q], (f4){0.f, 0.f, 0.f, 0.f}); d[2 * q + 1] = mfma(a0, uv[2 * q + 1], (f4){0.f, 0.f, 0.f, 0.f});
      d[2 * q] = mfma(a1, uv[8 + 2 * q], d[2 * q]); d[2 * q + 1] = mfma(a1, uv[9 + 2 * q], d[2 * q + 1]);
    }
#pragma unroll
    for (int nt = 0; nt < 8; ++nt) { const f2 y = w01 * (f2){d[nt][0], d[nt][1]} + w23 * (f2){d[nt][2], d[nt][3]}; o[nt] = y[0] + y[1]; }
    const float u0 = sum32(o[0], o[4]), u1 = sum32(o[1], o[5]), u2 = sum32(o[2], o[6]), u3 = sum32(o[3], o[7]);
    const float v0 = sum16(u0, u2), v1 = sum16(u1, u3);
    LDS_WRF(rw, v0); LDS_WRF(rw + 64, v1);
    BAR();
    if (w == 0) {
      const f4 a = (__builtin_bit_cast(f4, LDS_RD128(rd)) + __builtin_bit_cast(f4, LDS_RD128(rd + 512))) + (__builtin_bit_cast(f4, LDS_RD128(rd + 1024)) + __builtin_bit_cast(f4, LDS_RD128(rd + 1536)));
      st8(ws, o5, (v2u){nosent(pk(a[0], a[1])), nosent(pk(a[2], a[3]))});
    }
    STAMP(10);
  }
  if (rc >= 8) {
    const int row = ((rc - 8) * 8 + w) * 27 + (lane >> 1);
    if (w != 0) __builtin_amdgcn_s_sleep(6);
    l2pf(mkrs(p.w2, FF * NM * 2), (lane < 54) ? row * NM * 2 + ((160 * xcd) & ~127) + (lane & 1) * 128 : 0x7ffffff0, L_SCR);
  }
  if (r6) {
    const int bb = bb6;
    float xv[4], gav[4], cbv[4];
    float b1a = blo(b1v), b1b = bhi(b1v);
    if (w < 5) {
      v4u ga, sb, cbb;
      const int om = WS_MV + myc + mq * 16;
      for (unsigned sp = 0; sp < SPIN_CAP; ++sp) { RELOAD(); ga = ld16(ws, om); sb = ld16(ws, om + 2560); cbb = ld16(ws, om + 5120); if (__all(nos4(ga) & nos4(sb) & nos4(cbb))) break; }
      STAMP(22);
      const float mu0 = LDS_RDF(L_MU + bb * 4);
      xv[0] = blo(xx.x) - mu0; xv[1] = bhi(xx.x) - mu0; xv[2] = blo(xx.y) - mu0; xv[3] = bhi(xx.y) - mu0;
      float sbv[4];
#pragma unroll
      for (int k = 0; k < 4; ++k) { gav[k] = bb ? bhi(ga[k]) : blo(ga[k]); sbv[k] = bb ? bhi(sb[k]) : blo(sb[k]); cbv[k] = rbf(1.f + (bb ? bhi(cbb[k]) : blo(cbb[k]))); }
      LDS_WR64(L_GS + bb * 1280 + mq * 8, ((v2u){pk(cbv[0], cbv[1]), pk(cbv[2], cbv[3])}));
      LDS_WR64(L_GS + 2560 + bb * 1280 + mq * 8, ((v2u){pk(sbv[0], sbv[1]), pk(sbv[2], sbv[3])}));
    }
    BAR();
    {
      f4 d = {0.f, 0.f, 0.f, 0.f};
      v4u gf[5];
#pragma unroll
      for (int ks = 0; ks < 5; ++ks) gf[ks] = LDS_RD128(L_GS + (i & 3) * 1280 + 320 * (w >> 1) + g4 * 16 + ks * 64);
      SCHED();
#pragma unroll
      for (int ks = 0; ks < 5; ++ks) d = mfma(gf[ks], w1f[ks], d);
      if (lane < 16) {
#pragma unroll
        for (int q = 0; q < 4; ++q) LDS_WRF(L_GS + 5120 + (((w >> 1) * 4 + q) * 32 + (w & 1) * 16 + lane) * 4, d[q]);
      }
    }
    const int o6 = WS_H6 + myc + bb * 1280 + mq * 8;
    int scw = L_SC + bb * 1280 + mq * 8, scr = L_SC + (i & 1) * 1280 + 320 * (w >> 1) + g4 * 16, str = L_CRED + ((t >> 4) & 1) * 64;
    int stw = L_CRED + bb * 64 + ((t >> 5) - 5 * bb) * 8, rw = L_CRED + 128 + ((w >> 1) * 64 + (w & 1) * 16 + i) * 4, rd = L_CRED + 128 + 2 * (t & 31) * 4;
    int o7 = WS_H7 + 128 * t6 + (t & 28) * 4 + (t & 1) * 8192;
    PIN(scw); PIN(scr); PIN(str); PIN(o7); PIN(stw); PIN(rw); PIN(rd); PIN(b1a); PIN(b1b);
    int gsr = L_GS + 5120 + (((t >> 4) & 1) * 32 + 2 * (t & 15)) * 4; PIN(gsr);
#pragma unroll
    for (int k = 0; k < 4; ++k) { PIN(gav[k]); PIN(cbv[k]); PIN(xv[k]); }
#pragma unroll
    for (int ks = 0; ks < 5; ++ks) PIN(w1f[ks]);
    STAMP(11);
    float z[4] = {0.f, 0.f, 0.f, 0.f};
    if (w < 5) {
      float a[4];
      for (unsigned sp = 0; sp < SPIN_CAP; ++sp) {
        RELOAD();
        v2u pa[8];
        unsigned mm = 0;
#pragma unroll
        for (int k = 0; k < 8; ++k) pa[k] = ld8(ws, o6 + (k >> 1) * 16384 + (k & 1) * 4096);
        a[0] = a[1] = a[2] = a[3] = 0.f;
#pragma unroll
        for (int k = 0; k < 8; ++k) { mm = max(mm, max(pa[k].x, pa[k].y)); a[0] += blo(pa[k].x); a[1] += bhi(pa[k].x); a[2] += blo(pa[k].y); a[3] += bhi(pa[k].y); }
        PIN(a[0]); PIN(a[1]); PIN(a[2]); PIN(a[3]);
        if (__all(mm != SENT)) break;
      }
      STAMP(12);
#pragma unroll
      for (int k = 0; k < 4; ++k) z[k] = fmaf(gav[k], a[k], xv[k]);
      LDS_WR64(scw, ((v2u){pk(z[0] * cbv[0], z[1] * cbv[1]), pk(z[2] * cbv[2], z[3] * cbv[3])}));
    }
    BAR();
    v4u hf[5];
#pragma unroll
    for (int ks = 0; ks < 5; ++ks) hf[ks] = LDS_RD128(scr + ks * 64);
    v2u gv[4], sv[4];
#pragma unroll
    for (int k = 0; k < 4; ++k) { gv[k] = LDS_RD64(gsr + k * 512); sv[k] = LDS_RD64(gsr + k * 512 + 256); }
    SCHED();
    const float s1 = rsum32((z[0] + z[1]) + (z[2] + z[3])), s2 = rsum32((z[0] * z[0] + z[1] * z[1]) + (z[2] * z[2] + z[3] * z[3]));
    f4 d = {0.f, 0.f, 0.f, 0.f}, d2 = d;
    d = mfma(hf[0], w1f[0], d); d2 = mfma(hf[1], w1f[1], d2); d = mfma(hf[2], w1f[2], d); d2 = mfma(hf[3], w1f[3], d2); d = mfma(hf[4], w1f[4], d);
    const float gs0 = (__uint_as_float(gv[0].x) + __uint_as_float(gv[1].x)) + (__uint_as_float(gv[2].x) + __uint_as_float(gv[3].x));
    const float gs1 = (__uint_as_float(gv[0].y) + __uint_as_float(gv[1].y)) + (__uint_as_float(gv[2].y) + __uint_as_float(gv[3].y));
    b1a += (__uint_as_float(sv[0].x) + __uint_as_float(sv[1].x)) + (__uint_as_float(sv[2].x) + __uint_as_float(sv[3].x));
    b1b += (__uint_as_float(sv[0].y) + __uint_as_float(sv[1].y)) + (__uint_as_float(sv[2].y) + __uint_as_float(sv[3].y));
    if (w < 5 && (lane & 31) == 0) LDS_WR64(stw, ((v2u){__float_as_uint(s1), __float_as_uint(s2)}));
    if (lane < 16) { LDS_WRF(rw, d[0] + d2[0]); LDS_WRF(rw + 128, d[1] + d2[1]); }
    BAR();
    if (w == 0) {
      v2u v[4];
      const f4 sa = __builtin_bit_cast(f4, LDS_RD128(str)), sbq = __builtin_bit_cast(f4, LDS_RD128(str + 16));
      const v2u sc_ = LDS_RD64(str + 32);
#pragma unroll
      for (int k = 0; k < 4; ++k) v[k] = LDS_RD64(rd + k * 256);
      SCHED();
      const float dl = (((sa[0] + sa[2]) + (sbq[0] + sbq[2])) + __uint_as_float(sc_.x)) * (1.f / NM);
      const float q2 = (((sa[1] + sa[3]) + (sbq[1] + sbq[3])) + __uint_as_float(sc_.y)) * (1.f / NM);
      const float rs = __builtin_amdgcn_rsqf(fmaxf(fmaf(-dl, dl, q2), 0.f) + 1e-5f);
      const float a0 = (__uint_as_float(v[0].x) + __uint_as_float(v[1].x)) + (__uint_as_float(v[2].x) + __uint_as_float(v[3].x));
      const float a1 = (__uint_as_float(v[0].y) + __uint_as_float(v[1].y)) + (__uint_as_float(v[2].y) + __uint_as_float(v[3].y));
      const float r0 = fmaxf(fmaf(rs, fmaf(-dl, gs0, a0), b1a), 0.f), r1 = fmaxf(fmaf(rs, fmaf(-dl, gs1, a1), b1b), 0.f);
      const v4u o = quad_gather(nosent(pk(r0 * r0, r1 * r1)));
      if (t < 32 && (t & 3) < 2) st16(ws, o7, o);
    }
    STAMP(13);
  }
}

__global__ void __launch_bounds__(512, 2) fused_decode(Params p) {
  extern __shared__ __attribute__((aligned(16))) unsigned char lds_raw[];
  LAS unsigned char* lds = (LAS unsigned char*)(unsigned)0;
  asm volatile("" :: "s"((unsigned)(size_t)(LAS unsigned char*)lds_raw));
  const int g = blockIdx.x, w = __builtin_amdgcn_readfirstlane(threadIdx.x >> 6), lane = threadIdx.x & 63;
  if (w == 7) {
    unsigned long long pc;
    asm volatile("s_getpc_b64 %0" : "=s"(pc));
    const rsrc_t rc = __builtin_amdgcn_make_buffer_rsrc((void*)pc, 0, CODE_LEN, 0x00020000);
    const int vo = ((g >> 3) + 32 * (lane & 15)) * 128;
    asm volatile("s_mov_b32 m0, %0\n\ts_nop 0\n\tbuffer_load_dword %1, %2, 0 offen lds" :: "s"(L_SCR), "v"(lane < 16 ? vo : 0x7ffffff0), "s"(rc) : "m0", "memory");
  }
  const rsrc_t ws = __builtin_amdgcn_make_buffer_rsrc(p.ws, 0, WS_BYTES, 0x00020000);
  const bool s0 = g < 100;
  const int cb = g % 20, ks0 = g / 20, t = threadIdx.x;
  unsigned tag0 = 0;
  if (s0) {
    const int so = (128 * ks0 * 3840 + 64 * cb) * 2 + w * 16 * 7680;
    const rsrc_t rm = mkrs(p.wmod, NM * 3840 * 2);
    if (w < 4) {
      unsigned e0, cv;
      int s0a = L_QS + tr_lane<128>(w, lane), s0s = L_PB + (lane & 1) * 256 + (lane >> 4) * 16, h1o = WS_H1 + ks0 * 10240 + (64 * cb + 16 * w + lane) * 8;
      asm volatile("buffer_load_ushort %1, %3, %4, %5 offen\n\tbuffer_load_dword %0, off, %2, 0\n\t"
                   "s_mov_b32 m0, %6\n\ts_nop 0\n\tbuffer_load_dwordx4 %7, %8, %9 offen nt lds\n\t"
                   "s_mov_b32 m0, %10\n\ts_nop 0\n\tbuffer_load_dwordx4 %11, %8, %12 offen nt lds"
                   : "=&v"(e0), "=&v"(cv)
                   : "s"(ws), "v"(((t >> 7) * NM + (t & 127)) * 2), "s"(mkrs(p.c, 2560)), "s"(256 * ks0),
                     "s"(L_QS + w * 2048), "v"(dma_lane<128>(7680, lane, 0)), "s"(rm), "s"(so),
                     "s"(L_QS + w * 2048 + 1024), "v"(dma_lane<128>(7680, lane, 1)), "s"(so + 8 * 7680)
                   : "m0", "memory");
      asm volatile("s_waitcnt vmcnt(2)" : "+v"(e0), "+v"(cv), "+v"(s0a), "+v"(s0s), "+v"(h1o) :: "memory");
      STAMP(1);
      tag0 = e0 + 1u;
      const float a = blo(cv);
      *(LAS u16*)(lds + L_PB + t * 2) = (u16)pk(a * __builtin_amdgcn_rcpf(1.f + __expf(-a)), 0.f);
      VMW(0);
      STAMP(16);
      BAR();
      STAMP(2);
      f4 acc = {0.f, 0.f, 0.f, 0.f}, acc2 = acc;
      v4u bf[4], sa[4];
#pragma unroll
      for (int ks = 0; ks < 4; ++ks) { bf[ks] = trfrag<128>(lds, s0a + ks * 4096); sa[ks] = LDS_RD128(s0s + ks * 64); }
      SCHED();
      acc = mfma(sa[0], bf[0], acc); acc2 = mfma(sa[1], bf[1], acc2); acc = mfma(sa[2], bf[2], acc); acc2 = mfma(sa[3], bf[3], acc2);
      if (lane < 16) st8(ws, h1o, (v2u){pk(acc[0] + acc2[0], acc[1] + acc2[1]), tag0});
      STAMP(17);
    } else {
      dma(rm, dma_lane<128>(7680, lane, 0), so, L_QS + w * 2048);
      dma(rm, dma_lane<128>(7680, lane, 1), so + 8 * 7680, L_QS + w * 2048 + 1024);
      VMW(0);
      BAR();
    }
  } else if (g < 128) {
    if (w == 0) tag0 = ldE(ws) + 1u;
    if (w == 1) {
      const v4u sv = {SENT, SENT, SENT, SENT};
#pragma unroll 1
      for (int n = 0; n < 10; ++n) { const int ch = (g - 100) + 28 * n; if (ch < 256) st16(ws, WS_CLR + ch * 1024 + lane * 16, sv); }
      const unsigned tg = ldE(ws) + 1u;
      VMW(0);
      if (lane < 2) st8(ws, WS_CF + lane * 8192 + (g - 100) * 128 + 8, (v2u){tg, 0u});
      STAMPW(23, 1);
    } else if (w == 2) {
      unsigned tg;
      const int pa = WS_PR + (g - 100) * 128;
      unsigned long long t0, t1, t2, t3;
      if (lane == 0) { st4(ws, pa, 0u); st4(ws, pa + 8192, 0u); }
      asm volatile("s_waitcnt vmcnt(1)\n\ts_memtime %0\n\ts_waitcnt vmcnt(0)\n\ts_memtime %1\n\ts_waitcnt lgkmcnt(0)" : "=&s"(t0), "=&s"(t1) :: "memory");
      if (lane == 0) { st4(ws, pa + 8192 + 64, 0u); st4(ws, pa + 64, 0u); }
      asm volatile("buffer_load_dword %2, off, %3, 0\n\ts_waitcnt vmcnt(2)\n\ts_memtime %0\n\ts_waitcnt vmcnt(1)\n\ts_memtime %1\n\ts_waitcnt vmcnt(0) lgkmcnt(0)" : "=&s"(t2), "=&s"(t3), "=&v"(tg) : "s"(ws) : "memory");
      const unsigned vote = (((unsigned)(t1 - t0) > (unsigned)(t3 - t2)) ? 0u : 1u) ^ ((g >> 2) & 1);
      if (lane < 2) st8(ws, WS_CF + lane * 8192 + (g - 100) * 128, (v2u){tg + 1u, vote});
      STAMPW(22, 2);
    }
    BAR();
  }
  if ((g >> 3) < 16) a_path(p, lds, ws, g, w, lane, tag0 ST_PASS); else c_path(p, lds, ws, g, w, lane ST_PASS);
}

extern "C" void kernel_launch(void* const* d_in, const int* in_sizes, int n_in,
                              void* d_out, int out_size, void* d_ws, size_t ws_size,
                              hipStream_t stream) {
  Params p;
  p.x = (const u16*)d_in[0]; p.ckv = (const u16*)d_in[1]; p.wq = (const u16*)d_in[2]; p.wuk = (const u16*)d_in[3];
  p.wuv = (const u16*)d_in[4]; p.wo = (const u16*)d_in[5]; p.w1 = (const u16*)d_in[7]; p.b1 = (const u16*)d_in[8];
  p.w2 = (const u16*)d_in[9]; p.b2 = (const u16*)d_in[10]; p.c = (const u16*)d_in[11]; p.wmod = (const u16*)d_in[12];
  p.out = (u16*)d_out; p.ws = (unsigned char*)d_ws;
  (void)hipFuncSetAttribute((const void*)fused_decode, hipFuncAttributeMaxDynamicSharedMemorySize, LDS_BYTES);
  hipLaunchKernelGGL(fused_decode, dim3(256), dim3(512), LDS_BYTES, stream, p);
}
```

```cpp
#include <hip/hip_runtime.h>
#include <hip/hip_bf16.h>
#include <stdint.h>

#define LAS __attribute__((address_space(3)))
#define GAS __attribute__((address_space(1)))
typedef unsigned short u16;
typedef unsigned v4u __attribute__((ext_vector_type(4)));
typedef unsigned v2u __attribute__((ext_vector_type(2)));
typedef float f4 __attribute__((ext_vector_type(4)));
typedef float f2 __attribute__((ext_vector_type(2)));
typedef short s8v __attribute__((ext_vector_type(8)));
typedef short s4v __attribute__((ext_vector_type(4)));
typedef __bf16 bf2v __attribute__((ext_vector_type(2)));
typedef int i4v __attribute__((ext_vector_type(4)));
typedef unsigned short us8v __attribute__((ext_vector_type(8)));
typedef __amdgpu_buffer_rsrc_t rsrc_t;

constexpr int SK = 4096, NM = 640, DC = 512, FF = 1728, HD = 4096;

constexpr int DLY_WQ = 6;
constexpr int INFL1 = 2, NSLOW = 6;
constexpr int INFL = 6;
constexpr int N3 = 8;
constexpr int CODE_LEN = 23552;

constexpr int WS_E = 0;
constexpr int WS_PR = 81920;
constexpr int WS_CF = 4096;
constexpr int WS_H1 = 16384;
constexpr int WS_H2 = 180224;
constexpr int WS_H3 = 442368;
constexpr int WS_ST = 507904;
constexpr int WS_H5 = 524288;
constexpr int WS_H6 = 589824;
constexpr int WS_H7 = 655360;
constexpr int WS_MV = 671744;
constexpr int WS_CLR = WS_H2;
constexpr int WS_H4 = 2097152;
constexpr int WS_BYTES = 4 << 20;
constexpr size_t WS_OFF = 16 << 20;
constexpr unsigned SENT = 0xFFFFFFFFu;
constexpr unsigned SPIN_CAP = 30000u;

constexpr int L_CKV = 0, L_QS = 131072, L_AM = 147456;
constexpr int L_W2 = 81920;
constexpr int L_MX = L_AM, L_PB = L_AM + 512, L_ARED = L_AM + 4608, L_ASC = L_AM + 8704;
constexpr int L_WQ = 0, L_WUV = 0, L_UK = 40960, L_ROLE = 40960, L_CM = 122880;
constexpr int L_HS = L_CM, L_QH = L_CM + 640, L_SC = L_CM + 1280, L_CRED = L_CM + 3840, L_GS = L_CM + 8192, L_MU = L_CM + 1152;
constexpr int LDS_BYTES = 163840, L_SCR = LDS_BYTES - 512, L_FLIP = LDS_BYTES - 528;

struct Params {
  const u16 *x, *ckv, *wq, *wuk, *wuv, *wo, *w1, *b1, *w2, *b2, *c, *wmod;
  u16* out;
  unsigned char* ws;
};

__device__ __forceinline__ float blo(unsigned w) { return __uint_as_float(w << 16); }
__device__ __forceinline__ float bhi(unsigned w) { return __uint_as_float(w & 0xffff0000u); }
__device__ __forceinline__ unsigned pk(float lo, float hi) { f2 f = {lo, hi}; return __builtin_bit_cast(unsigned, __builtin_convertvector(f, bf2v)); }
__device__ __forceinline__ float rbf(float f) { return blo(pk(f, 0.f)); }
__device__ __forceinline__ unsigned nosent(unsigned v) { return v < 0xFFFFFFFEu ? v : 0xFFFFFFFEu; }
__device__ __forceinline__ f4 mfma(v4u a, v4u b, f4 c) { return __builtin_amdgcn_mfma_f32_16x16x32_bf16(__builtin_bit_cast(s8v, a), __builtin_bit_cast(s8v, b), c, 0, 0, 0); }
template <int C> __device__ __forceinline__ float dpp(float x) { return __builtin_bit_cast(float, __builtin_amdgcn_mov_dpp(__builtin_bit_cast(int, x), C, 0xf, 0xf, true)); }
template <int C> __device__ __forceinline__ unsigned dppu(unsigned x) { return (unsigned)__builtin_amdgcn_mov_dpp((int)x, C, 0xf, 0xf, true); }
__device__ __forceinline__ float sum16(float a, float b) { auto s = __builtin_amdgcn_permlane16_swap(__float_as_uint(a), __float_as_uint(b), false, false); return __uint_as_float(s[0]) + __uint_as_float(s[1]); }
__device__ __forceinline__ float sum32(float a, float b) { auto s = __builtin_amdgcn_permlane32_swap(__float_as_uint(a), __float_as_uint(b), false, false); return __uint_as_float(s[0]) + __uint_as_float(s[1]); }
__device__ __forceinline__ float vmax(float a, float b) { return __builtin_elementwise_maximum(a, b); }
__device__ __forceinline__ float vmax3(float a, float b, float c) { return vmax(vmax(a, b), c); }
__device__ __forceinline__ float max16(float a) { auto s = __builtin_amdgcn_permlane16_swap(__float_as_uint(a), __float_as_uint(a), false, false); return vmax(__uint_as_float(s[0]), __uint_as_float(s[1])); }
__device__ __forceinline__ float max32(float a) { auto s = __builtin_amdgcn_permlane32_swap(__float_as_uint(a), __float_as_uint(a), false, false); return vmax(__uint_as_float(s[0]), __uint_as_float(s[1])); }
__device__ __forceinline__ float rsum32(float v) {
  v += dpp<0xB1>(v); v += dpp<0x4E>(v); v += dpp<0x141>(v); v += dpp<0x140>(v); return sum16(v, v);
}
__device__ __forceinline__ float rdl(float v, int l) { return __builtin_bit_cast(float, __builtin_amdgcn_readlane(__builtin_bit_cast(int, v), l)); }

__device__ __forceinline__ rsrc_t mkrs(const void* p, int bytes) { return __builtin_amdgcn_make_buffer_rsrc((void*)p, 0, bytes, 0x00020000); }
__device__ __forceinline__ v4u ld16(rsrc_t r, int off) { return __builtin_amdgcn_raw_buffer_load_b128(r, off, 0, 16); }
__device__ __forceinline__ v2u ld8(rsrc_t r, int off) { return __builtin_amdgcn_raw_buffer_load_b64(r, off, 0, 16); }
__device__ __forceinline__ unsigned ld4(rsrc_t r, int off) { return __builtin_amdgcn_raw_buffer_load_b32(r, off, 0, 16); }
__device__ __forceinline__ void st16(rsrc_t r, int off, v4u v) { __builtin_amdgcn_raw_buffer_store_b128(v, r, off, 0, 16); }
__device__ __forceinline__ void st8(rsrc_t r, int off, v2u v) { __builtin_amdgcn_raw_buffer_store_b64(v, r, off, 0, 16); }
__device__ __forceinline__ void st4(rsrc_t r, int off, unsigned v) { __builtin_amdgcn_raw_buffer_store_b32(v, r, off, 0, 16); }
__device__ __forceinline__ v4u ld16s(rsrc_t r, int off, int so) { return __builtin_amdgcn_raw_buffer_load_b128(r, off, so, 16); }
__device__ __forceinline__ void st16s(rsrc_t r, int off, int so, v4u v) { __builtin_amdgcn_raw_buffer_store_b128(v, r, off, so, 16); }
__device__ __forceinline__ v4u gld16(const void* p) { return *(const GAS v4u*)p; }
__device__ __forceinline__ unsigned mktag(unsigned e) { return e + 1u == SENT ? 0u : e + 1u; }
__device__ __forceinline__ unsigned ldtag(rsrc_t r) { return mktag(__builtin_amdgcn_raw_buffer_load_b32(r, WS_E, 0, 0)); }
__device__ __forceinline__ void dma(rsrc_t r, int voff, int soff, int lds_dst) {
  asm volatile("s_mov_b32 m0, %0\n\ts_nop 0\n\tbuffer_load_dwordx4 %1, %2, %3 offen lds" :: "s"(lds_dst), "v"(voff), "s"(r), "s"(soff) : "m0", "memory");
}
__device__ __forceinline__ void l2pf(rsrc_t r, int voff, int scr) {
  asm volatile("s_mov_b32 m0, %0\n\ts_nop 0\n\tbuffer_load_dword %1, %2, 0 offen lds" :: "s"(scr), "v"(voff), "s"(r) : "m0", "memory");
}
#define BAR() asm volatile("s_waitcnt lgkmcnt(0)\n\ts_barrier" ::: "memory")
#define VMW(n) asm volatile("s_waitcnt vmcnt(%0)" :: "n"(n) : "memory")
#define RELOAD() asm volatile("" ::: "memory")
#define PIN(v) asm volatile("" : "+v"(v))
#define SCHED() __builtin_amdgcn_sched_barrier(0)
__device__ __forceinline__ unsigned mx4(v4u v) { return max(max(v.x, v.y), max(v.z, v.w)); }
__device__ __forceinline__ bool nos4(v4u v) { return mx4(v) != SENT; }
__device__ __forceinline__ int fsw(int k) { return (k & 3) ^ ((k & 4) ? 12 : 0) ^ ((k & 8) ? 4 : 0); }

#define LDS_RD128(off) (*(const LAS v4u*)(lds + (off)))
#define LDS_RD64(off) (*(const LAS v2u*)(lds + (off)))
#define LDS_RDF(off) (*(const LAS float*)(lds + (off)))
#define LDS_WR128(off, v) (*(LAS v4u*)(lds + (off)) = (v))
#define LDS_WR64(off, v) (*(LAS v2u*)(lds + (off)) = (v))
#define LDS_WR32(off, v) (*(LAS unsigned*)(lds + (off)) = (v))
#define LDS_WRF(off, v) (*(LAS float*)(lds + (off)) = (v))

template <int RB> __device__ __forceinline__ int swz(int row) {
  if (RB == 256) return 2 * ((row & 3) | (((row >> 3) & 1) << 2));
  if (RB == 128) return 2 * (((row >> 1) & 1) | (((row >> 3) & 1) << 1));
  if (RB == 64) return 2 * ((row >> 3) & 1);
  return 0;
}
__device__ __forceinline__ v2u trrd(LAS unsigned char* lds, int off) { return __builtin_bit_cast(v2u, __builtin_amdgcn_ds_read_tr16_b64_v4i16((LAS s4v*)(lds + off))); }
template <int RB> __device__ __forceinline__ int tr_lane(int ctile, int lane) {
  const int g4 = lane >> 4, q = (lane & 15) >> 2, p = lane & 3, r0 = 8 * g4 + q;
  if (RB == 32) return (r0 ^ ((g4 & 1) << 2)) * 32 + p * 8;
  return r0 * RB + (((2 * ctile + (p >> 1)) ^ swz<RB>(r0)) << 4) + (p & 1) * 8;
}
template <int RB> __device__ __forceinline__ v4u trfrag(LAS unsigned char* lds, int off) {
  v2u a = trrd(lds, off), b = trrd(lds, RB == 32 ? (off ^ 128) : off + 4 * RB);
  return (v4u){a.x, a.y, b.x, b.y};
}
template <int RB> __device__ __forceinline__ int dma_lane(int ld, int lane, int par) {
  constexpr int CPR = RB / 16;
  const int r = lane / CPR, pos = lane % CPR;
  if (RB == 32) return (r ^ (((r >> 3) & 1) << 2)) * ld + pos * 16;
  return r * ld + ((pos ^ swz<RB>(r + 8 * par)) << 4);
}
__device__ __forceinline__ v4u silu8(v4u c) {
  v4u o;
#pragma unroll
  for (int i = 0; i < 4; ++i) { float a = blo(c[i]), b = bhi(c[i]); o[i] = pk(a * __builtin_amdgcn_rcpf(1.f + __expf(-a)), b * __builtin_amdgcn_rcpf(1.f + __expf(-b))); }
  return o;
}
__device__ __forceinline__ v4u quad_gather(unsigned v) { return (v4u){dppu<0x00>(v), dppu<0x55>(v), dppu<0xAA>(v), dppu<0xFF>(v)}; }

__device__ __forceinline__ unsigned poll_votes(rsrc_t ws, unsigned tag, int lane) {
  const int off = WS_CF + (lane >> 5) * 8192 + min(lane & 31, 27) * 128;
  unsigned long long ones = 0;
  for (unsigned sp = 0; sp < SPIN_CAP; ++sp) {
    RELOAD();
    const v4u v = ld16(ws, off);
    const unsigned long long ok = __ballot(v.x == tag), ok2 = __ballot(v.z == tag);
    ones = __ballot((v.x == tag) & (v.y == 1u));
    if ((((unsigned)ok | (unsigned)(ok >> 32)) & ((unsigned)ok2 | (unsigned)(ok2 >> 32))) == 0xFFFFFFFFu) break;
  }
  return __builtin_popcount(((unsigned)ones | (unsigned)(ones >> 32)) & 0x0FFFFFFFu) >= 14 ? 1u : 0u;
}

__device__ __forceinline__ void a_path(const Params& p, LAS unsigned char* lds, rsrc_t ws, int g, int w, int lane, unsigned tag) {
  const int t = threadIdx.x, xcd = g & 7, r = g >> 3, b = xcd >> 2, s = (xcd & 3) * 8 + (r >> 1), hg = r & 1;
  const int i = lane & 15, g4 = lane >> 4;
  const rsrc_t rk = mkrs(p.ckv + ((size_t)b * SK + s * 128) * DC, 131072);
  const int l16 = lane << 4, rot = hg * 64;
#define CKV_ROW(k_) do { const int kk_ = (k_); dma(rk, l16 ^ (fsw(kk_ & 15) << 4), ((kk_ + rot) & 127) * 1024, L_CKV + kk_ * 1024); } while (0)
  if (w >= 4) {
    __builtin_amdgcn_s_sleep(1);
    l2pf(mkrs(p.wuk, DC * HD * 2), (r * 32 + (w - 4) * 8 + (lane >> 3)) * HD * 2 + xcd * 1024 + (lane & 7) * 128, L_SCR);
  }
  __builtin_amdgcn_s_sleep(1);
#pragma unroll 1
  for (int n = 0; n < NSLOW; ++n) { VMW(INFL1 - 1); CKV_ROW(n * 8 + w); }
#pragma unroll 1
  for (int n = NSLOW; n < 16; ++n) { VMW(INFL - 1); CKV_ROW(n * 8 + w); }
  if (w == 0) { const unsigned f = poll_votes(ws, tag, lane); if (lane == 0) LDS_WR32(L_FLIP, f); }
  VMW(INFL);
  BAR();
  const int fl = __builtin_amdgcn_readfirstlane(*(const LAS unsigned*)(lds + L_FLIP)), myc = (b ^ fl) * 8192;
  int h4o = WS_H4 + ((4 * g4 * 4 + (w & 3)) * 2 + b) * 16384 + (hg ^ fl ^ 1) * 8192 + s * 256 + i * 16;
  {
    const v4u sv = {SENT, SENT, SENT, SENT};
    st16(ws, h4o + (w >> 2) * 262144, sv); st16(ws, h4o + (w >> 2) * 262144 + 131072, sv);
  }
  v4u fr[32], q0, q1;
  const int h3i = WS_H3 + (hg * 2) * 16384 + myc + t * 16;
  int qa[4];
#pragma unroll
  for (int k4 = 0; k4 < 4; ++k4) qa[k4] = i * 1024 + ((((4 * k4 + g4) ^ fsw(i))) << 4);
#pragma unroll
  for (int hf = 0; hf < 2; ++hf) {
    if (hf == 1) {
      VMW(0);
      BAR();
      q0 = ld16(ws, h3i); q1 = ld16(ws, h3i + 16384);
      SCHED();
    }
    if (w < 4) {
#pragma unroll
      for (int ks = 0; ks < 16; ++ks) fr[hf * 16 + ks] = LDS_RD128(L_CKV + (64 * hf + 16 * w) * 1024 + qa[ks & 3] + (ks >> 2) * 256);
    } else {
      const int pw = w - 4;
#pragma unroll
      for (int u = 0; u < 4; ++u) {
        v4u X[4];
#pragma unroll
        for (int jj = 0; jj < 4; ++jj) { const int kl = 4 * g4 + jj; X[jj] = LDS_RD128(L_CKV + (64 * hf + 16 * u + kl) * 1024 + pw * 256 + ((i ^ fsw(kl)) << 4)); }
#pragma unroll
        for (int tt = 0; tt < 8; ++tt)
#pragma unroll
          for (int m = 0; m < 2; ++m) fr[u * 8 + tt][2 * hf + m] = __builtin_amdgcn_perm(X[2 * m + 1][tt >> 1], X[2 * m][tt >> 1], (tt & 1) ? 0x07060302u : 0x05040100u);
      }
    }
    if (hf == 0) {
#pragma unroll
      for (int n = 0; n < 32; ++n) PIN(fr[n]);
    }
  }
#pragma unroll
  for (int n = 0; n < 32; ++n) PIN(fr[n]);
#pragma unroll
  for (int k4 = 0; k4 < 4; ++k4) { qa[k4] += L_QS; PIN(qa[k4]); }
  const bool r5 = r < 10, r7 = (r >= 10) & (r < 15);
  const int mt7 = xcd * 5 + (r - 10);
  const rsrc_t rr = r5 ? mkrs(p.wo, HD * NM * 2) : mkrs(p.wmod, NM * 3840 * 2);
  const int rld = r5 ? NM * 2 : 7680;
  int rl0 = dma_lane<128>(rld, lane, 0), rl1 = dma_lane<128>(rld, lane, 1);
  PIN(rl0); PIN(rl1);
  const int rso = r5 ? (512 * xcd * NM + 64 * r) * 2 : (1280 + 64 * mt7) * 2;
#define ROLE_INS(n_) do { const int in_ = (n_); dma(rr, (in_ & 1) ? rl1 : rl0, rso + in_ * 8 * rld, L_CKV + in_ * 1024); } while (0)
  int mxw = L_MX + i * 16 + (w & 3) * 4, mxr = L_MX + i * 16, pbw = L_PB + (w & 3) * 1024 + l16, pbr = L_PB + l16;
  PIN(h4o); PIN(mxw); PIN(mxr); PIN(pbw); PIN(pbr);
  {
    int d0 = L_QS + w * 1024 + ((lane ^ fsw(w)) << 4), d1 = L_QS + (w + 8) * 1024 + ((lane ^ fsw(w + 8)) << 4);
    PIN(d0); PIN(d1);
#pragma unroll 1
    for (unsigned sp = 0; sp < SPIN_CAP; ++sp) { if (__all(max(mx4(q0), mx4(q1)) != SENT)) break; RELOAD(); q0 = ld16(ws, h3i); q1 = ld16(ws, h3i + 16384); }
    LDS_WR128(d0, q0); LDS_WR128(d1, q1);
  }
#define RESCALE(u_) do { const unsigned dd_ = min((unsigned)(mrow - m4[u_]), 255u) * 0x00800080u; \
    pf[u_] = __builtin_bit_cast(v4u, __builtin_elementwise_sub_sat(__builtin_bit_cast(us8v, pf[u_]), __builtin_bit_cast(us8v, (v4u){dd_, dd_, dd_, dd_}))); } while (0)
  if (w < 4) {
    BAR();
    f4 sacc[2];
    sacc[0] = (f4){0.f, 0.f, 0.f, 0.f}; sacc[1] = sacc[0];
    v4u qf[16];
#pragma unroll
    for (int ks = 0; ks < 4; ++ks) qf[ks] = LDS_RD128(qa[ks]);
    SCHED();
#pragma unroll
    for (int ks = 4; ks < 12; ++ks) qf[ks] = LDS_RD128(qa[ks & 3] + (ks >> 2) * 256);
    SCHED();
#pragma unroll
    for (int ks = 0; ks < 4; ++ks) { sacc[0] = mfma(fr[ks], qf[ks], sacc[0]); sacc[1] = mfma(fr[16 + ks], qf[ks], sacc[1]); qf[12 + ks] = LDS_RD128(qa[ks] + 768); SCHED(); }
#pragma unroll
    for (int ks = 4; ks < 16; ++ks) { sacc[0] = mfma(fr[ks], qf[ks], sacc[0]); sacc[1] = mfma(fr[16 + ks], qf[ks], sacc[1]); }
    float mx = vmax3(vmax3(sacc[0][0], sacc[0][1], sacc[0][2]), vmax3(sacc[0][3], sacc[1][0], sacc[1][1]), vmax(sacc[1][2], sacc[1][3]));
    mx = __builtin_ceilf(max32(max16(mx)));
    float pp[8];
#pragma unroll
    for (int n = 0; n < 8; ++n) pp[n] = __builtin_amdgcn_exp2f(sacc[n >> 2][n & 3] - mx);
    LDS_WR32(mxw, (unsigned)(int)mx);
    LDS_WR128(pbw, ((v4u){pk(pp[0], pp[1]), pk(pp[2], pp[3]), pk(pp[4], pp[5]), pk(pp[6], pp[7])}));
    BAR();
    if (w == 0) {
      const i4v m4 = __builtin_bit_cast(i4v, LDS_RD128(mxr));
      v4u pf[4];
#pragma unroll
      for (int u = 0; u < 4; ++u) pf[u] = LDS_RD128(pbr + u * 1024);
      const int mrow = max(max(m4[0], m4[1]), max(m4[2], m4[3]));
      RESCALE(0); RESCALE(1); RESCALE(2); RESCALE(3);
      const v4u ones = {0x3F803F80u, 0x3F803F80u, 0x3F803F80u, 0x3F803F80u};
      f4 l4 = {0.f, 0.f, 0.f, 0.f};
#pragma unroll
      for (int u = 0; u < 4; ++u) l4 = mfma(ones, pf[u], l4);
      if (lane < 16) st8(ws, WS_ST + (hg ^ fl) * 8192 + (b * 32 + s) * 128 + lane * 8, (v2u){nosent(__float_as_uint((float)mrow)), nosent(__float_as_uint(l4[0]))});
    } else if (r7) {
      v4u cc;
      asm volatile("buffer_load_dwordx4 %0, %1, %2, 0 offen\n\ts_waitcnt vmcnt(0)" : "=&v"(cc) : "v"((t - 64) * 16), "s"(mkrs(p.c, 2560)) : "memory");
      LDS_WR128(L_ASC + (t - 64) * 16, silu8(cc));
    }
    BAR();
  } else {
    BAR();
    if (r5 | r7) {
#pragma unroll 1
      for (int n = w - 4; n < 4 * N3; n += 4) ROLE_INS(n);
    }
    int h4a = h4o + 131072, h4b = h4o + 262144, h4c = h4o + 393216;
    PIN(h4a); PIN(h4b); PIN(h4c);
    BAR();
    const i4v m4 = __builtin_bit_cast(i4v, LDS_RD128(mxr));
    v4u pf[4];
#pragma unroll
    for (int u = 0; u < 4; ++u) pf[u] = LDS_RD128(pbr + u * 1024);
    SCHED();
    const int mrow = max(max(m4[0], m4[1]), max(m4[2], m4[3]));
    f4 o[8];
    v4u ov[4];
    const f4 zz = {0.f, 0.f, 0.f, 0.f};
    RESCALE(0); SCHED();
    o[0] = mfma(pf[0], fr[0], zz); o[1] = mfma(pf[0], fr[1], zz); RESCALE(1); o[2] = mfma(pf[0], fr[2], zz); o[3] = mfma(pf[0], fr[3], zz); SCHED();
    o[0] = mfma(pf[1], fr[8], o[0]); o[1] = mfma(pf[1], fr[9], o[1]); RESCALE(2); o[2] = mfma(pf[1], fr[10], o[2]); o[3] = mfma(pf[1], fr[11], o[3]); SCHED();
    o[0] = mfma(pf[2], fr[16], o[0]); o[1] = mfma(pf[2], fr[17], o[1]); RESCALE(3); o[2] = mfma(pf[2], fr[18], o[2]); o[3] = mfma(pf[2], fr[19], o[3]); SCHED();
    o[0] = mfma(pf[3], fr[24], o[0]); o[1] = mfma(pf[3], fr[25], o[1]); o[2] = mfma(pf[3], fr[26], o[2]); o[3] = mfma(pf[3], fr[27], o[3]); SCHED();
#pragma unroll
    for (int u = 0; u < 4; ++u) { o[4] = mfma(pf[u], fr[u * 8 + 4], u ? o[4] : zz); o[5] = mfma(pf[u], fr[u * 8 + 5], u ? o[5] : zz); if (u == 1) SCHED(); if (u >= 2) { ov[2 * u - 4][0] = nosent(pk(o[0][2 * u - 4], o[1][2 * u - 4])); ov[2 * u - 3][0] = nosent(pk(o[0][2 * u - 3], o[1][2 * u - 3])); } }
    SCHED();
#pragma unroll
    for (int u = 0; u < 4; ++u) {
      o[6] = mfma(pf[u], fr[u * 8 + 6], u ? o[6] : zz); o[7] = mfma(pf[u], fr[u * 8 + 7], u ? o[7] : zz);
      if (u < 2) { ov[2 * u][1] = nosent(pk(o[2][2 * u], o[3][2 * u])); ov[2 * u + 1][1] = nosent(pk(o[2][2 * u + 1], o[3][2 * u + 1])); }
      else { ov[2 * u - 4][2] = nosent(pk(o[4][2 * u - 4], o[5][2 * u - 4])); ov[2 * u - 3][2] = nosent(pk(o[4][2 * u - 3], o[5][2 * u - 3])); }
      SCHED();
    }
    ov[0][3] = nosent(pk(o[6][0], o[7][0])); st16(ws, h4o, ov[0]);
    ov[1][3] = nosent(pk(o[6][1], o[7][1])); st16(ws, h4a, ov[1]);
    ov[2][3] = nosent(pk(o[6][2], o[7][2])); st16(ws, h4b, ov[2]);
    ov[3][3] = nosent(pk(o[6][3], o[7][3])); st16(ws, h4c, ov[3]);
    VMW(0);
    BAR();
  }
  if (r5) {
#pragma unroll 1
    for (int n = 4 * N3 + w; n < 64; n += 8) ROLE_INS(n);
    VMW(0);
    BAR();
    v4u wf[8];
#pragma unroll
    for (int mt = 0; mt < 4; ++mt) {
      const int a = L_CKV + 64 * w * 128 + tr_lane<128>(mt, lane);
      wf[mt] = trfrag<128>(lds, a); wf[4 + mt] = trfrag<128>(lds, a + 4096);
    }
#pragma unroll
    for (int n = 0; n < 8; ++n) PIN(wf[n]);
    v4u a0 = {0u, 0u, 0u, 0u}, a1 = a0;
    const int hsrc = 4 * xcd + (w >> 1);
    const int o5 = WS_H5 + (i >> 1) * 16384 + myc + (i & 1) * 4096 + (hsrc & 15) * 256 + (w & 1) * 128 + g4 * 16;
    int rw = L_ARED + ((w * 2 + g4) * 64 + i) * 4, rd = L_ARED + ((t >> 5) * 64 + 2 * (t & 31)) * 4;
    int o6 = WS_H6 + (xcd >> 1) * 16384 + (xcd & 1) * 4096 + ((t >> 5) & 1) * 1280 + r * 128 + (t & 28) * 4 + (t & 1) * 8192;
    PIN(rw); PIN(rd); PIN(o6);
    for (unsigned sp = 0; sp < SPIN_CAP; ++sp) { RELOAD(); if (i < 8) { a0 = ld16(ws, o5); a1 = ld16(ws, o5 + 64); } if (__all(max(mx4(a0), mx4(a1)) != SENT)) break; }
    f4 d[4];
#pragma unroll
    for (int mt = 0; mt < 4; ++mt) d[mt] = mfma(a0, wf[mt], (f4){0.f, 0.f, 0.f, 0.f});
#pragma unroll
    for (int mt = 0; mt < 4; ++mt) d[mt] = mfma(a1, wf[4 + mt], d[mt]);
#pragma unroll
    for (int mt = 0; mt < 4; ++mt) {
      const float v = sum16(d[mt][0] + d[mt][2], d[mt][1] + d[mt][3]);
      if (lane < 32) LDS_WRF(rw + 64 * mt, v);
    }
    BAR();
    if (w == 0) {
      v2u v[8];
#pragma unroll
      for (int ww = 0; ww < 8; ++ww) v[ww] = LDS_RD64(rd + ww * 512);
      const float s0_ = ((__uint_as_float(v[0].x) + __uint_as_float(v[1].x)) + (__uint_as_float(v[2].x) + __uint_as_float(v[3].x))) + ((__uint_as_float(v[4].x) + __uint_as_float(v[5].x)) + (__uint_as_float(v[6].x) + __uint_as_float(v[7].x)));
      const float s1_ = ((__uint_as_float(v[0].y) + __uint_as_float(v[1].y)) + (__uint_as_float(v[2].y) + __uint_as_float(v[3].y))) + ((__uint_as_float(v[4].y) + __uint_as_float(v[5].y)) + (__uint_as_float(v[6].y) + __uint_as_float(v[7].y)));
      const v4u o = quad_gather(nosent(pk(s0_, s1_)));
      if ((lane & 3) < 2) st16(ws, o6, o);
    }
  } else if (r7) {
#pragma unroll 1
    for (int n = 4 * N3 + w; n < 80; n += 8) ROLE_INS(n);
    VMW(0);
    BAR();
    {
      f4 d = {0.f, 0.f, 0.f, 0.f};
      const int a = L_CKV + (w >> 2) * 40960 + tr_lane<128>(w & 3, lane), sa = L_ASC + (i & 1) * 1280 + (w >> 2) * 640 + g4 * 16;
      v4u af[10], bf[10];
#pragma unroll
      for (int ks = 0; ks < 10; ++ks) { af[ks] = LDS_RD128(sa + ks * 64); bf[ks] = trfrag<128>(lds, a + ks * 4096); }
      f4 d2 = d;
#pragma unroll
      for (int ks = 0; ks < 5; ++ks) { d = mfma(af[2 * ks], bf[2 * ks], d); d2 = mfma(af[2 * ks + 1], bf[2 * ks + 1], d2); }
      d += d2;
      if (w >= 4 && lane < 16) LDS_WR64(L_ARED + ((w - 4) * 16 + lane) * 8, ((v2u){__float_as_uint(d[0]), __float_as_uint(d[1])}));
      BAR();
      if (w < 4) {
        const v2u o = LDS_RD64(L_ARED + (w * 16 + i) * 8);
        const int n = 64 * mt7 + 16 * w + i;
        if (lane < 32) st4(ws, WS_MV + (n >= 1920 ? 16384 + (n - 1920) * 4 : n * 4) + g4 * 8192, nosent(pk(d[0] + __uint_as_float(o.x), d[1] + __uint_as_float(o.y))));
        VMW(0);
      }
      BAR();
    }
    {
      const rsrc_t r2 = mkrs(p.w2, FF * NM * 2);
      const int l2 = dma_lane<32>(NM * 2, lane, 0), rot2 = (mt7 & 3) * 14;
#pragma unroll 1
      for (int n = w; n < 54; n += 8) { int in = n + rot2; if (in >= 54) in -= 54; dma(r2, l2, 32 * mt7 + in * 32 * NM * 2, L_W2 + in * 1024); }
    }
    VMW(0);
    BAR();
    v4u wf[7];
    {
      const int a = L_W2 + w * 1024 + tr_lane<32>(0, lane);
#pragma unroll
      for (int n = 0; n < 6; ++n) wf[n] = trfrag<32>(lds, a + n * 8192);
      wf[6] = trfrag<32>(lds, a + (w < 6 ? 6 * 8192 : 0));
    }
#pragma unroll
    for (int n = 0; n < 7; ++n) PIN(wf[n]);
    float x1v = 0.f, gbv = 0.f, b2v = 0.f;
    if (w == 0) {
      const int bb = g4 & 1, m = 16 * mt7 + i;
      const float xv = blo(p.x[bb * NM + m]); b2v = blo(p.b2[m]);
      unsigned ga, gb, pa[8];
      for (unsigned sp = 0; sp < SPIN_CAP; ++sp) { RELOAD(); ga = ld4(ws, WS_MV + myc + m * 4); gb = ld4(ws, WS_MV + 16384 + myc + m * 4); if (__all((ga != SENT) & (gb != SENT))) break; }
      for (unsigned sp = 0; sp < SPIN_CAP; ++sp) {
        RELOAD();
        bool ok = true;
#pragma unroll
        for (int k = 0; k < 8; ++k) pa[k] = ld4(ws, WS_H6 + (k >> 1) * 16384 + myc + (k & 1) * 4096 + bb * 1280 + (m >> 1) * 4);
#pragma unroll
        for (int k = 0; k < 8; ++k) ok &= pa[k] != SENT;
        if (__all(ok)) break;
      }
      float a = 0.f;
#pragma unroll
      for (int k = 0; k < 8; ++k) a += (m & 1) ? bhi(pa[k]) : blo(pa[k]);
      x1v = rbf(xv + rbf((bb ? bhi(ga) : blo(ga)) * rbf(a)));
      gbv = bb ? bhi(gb) : blo(gb);
      PIN(x1v); PIN(gbv); PIN(b2v);
    }
    v4u af[7];
#pragma unroll
    for (int n = 0; n < 7; ++n) af[n] = (v4u){0u, 0u, 0u, 0u};
    const int o7 = WS_H7 + myc + w * 128 + (i & 1) * 64 + g4 * 16, o76 = o7 + (w < 6 ? 6 * 1024 : 0);
    for (unsigned sp = 0; sp < SPIN_CAP; ++sp) {
      RELOAD();
      if (i < 2) {
#pragma unroll
        for (int n = 0; n < 6; ++n) af[n] = ld16(ws, o7 + n * 1024);
        af[6] = ld16(ws, o76);
      }
      unsigned mm = 0;
#pragma unroll
      for (int n = 0; n < 7; ++n) mm = max(mm, mx4(af[n]));
      if (__all(mm != SENT)) break;
    }
    f4 d = {0.f, 0.f, 0.f, 0.f}, d2 = d;
#pragma unroll
    for (int n = 0; n < 3; ++n) { d = mfma(af[2 * n], wf[2 * n], d); d2 = mfma(af[2 * n + 1], wf[2 * n + 1], d2); }
    if (w < 6) d = mfma(af[6], wf[6], d);
    if (lane < 16) { LDS_WRF(L_ARED + (w * 32 + lane) * 4, d[0] + d2[0]); LDS_WRF(L_ARED + (w * 32 + 16 + lane) * 4, d[1] + d2[1]); }
    BAR();
    if (t < 32) {
      float v[8];
#pragma unroll
      for (int ww = 0; ww < 8; ++ww) v[ww] = LDS_RDF(L_ARED + (ww * 32 + t) * 4);
      const float a = ((v[0] + v[1]) + (v[2] + v[3])) + ((v[4] + v[5]) + (v[6] + v[7]));
      const float y = rbf(rbf(a) + b2v);
      p.out[(t >> 4) * NM + 16 * mt7 + (t & 15)] = (u16)pk(x1v + rbf(gbv * y), 0.f);
      if (mt7 == 0 && t == 0) st4(ws, WS_E, tag);
    }
  }
}

__device__ __forceinline__ void c_path(const Params& p, LAS unsigned char* lds, rsrc_t ws, int g, int w, int lane) {
  const int t = threadIdx.x, xcd = g & 7, rc = (g >> 3) - 16, h = xcd * 4 + (rc >> 2), j = rc & 3, iod = xcd >> 2;
  const int i = lane & 15, g4 = lane >> 4;
  const int p6 = xcd + 8 * (rc >> 1), t6 = 2 * p6 + (rc & 1);
  const bool r6 = p6 < 27;
  const unsigned tag = ldtag(ws);
  float ln0 = 0.f, ln1 = 0.f;
  v4u xa, xb, xc; unsigned xm = 0;
  const int dl0 = dma_lane<256>(HD * 2, lane, 0), dl1 = dma_lane<256>(HD * 2, lane, 1);
  if (w < 3) {
    const u16* xr = p.x + (lane >> 5) * NM + (lane & 31) * 8;
    xa = gld16(xr); xb = gld16(xr + 256); xc = gld16(xr + ((lane & 31) < 16 ? 512 : 0));
    xm = *(const GAS unsigned*)(p.x + (t & 1) * NM + 160 * j + min(t >> 1, 79) * 2);
  } else {
    const rsrc_t rq = mkrs(p.wq, NM * HD * 2);
    const int so = (160 * j * HD + h * 128) * 2 + (w - 3) * 32 * HD * 2;
    __builtin_amdgcn_s_sleep(DLY_WQ);
#pragma unroll
    for (int n = 0; n < 8; ++n) dma(rq, (n & 2) ? dl1 : dl0, so + n * 4 * HD * 2, L_WQ + (w - 3) * 8192 + n * 1024);
  }
  if (w < 3) {
    const bool v3 = (lane & 31) < 16;
    float sm = 0.f;
#pragma unroll
    for (int k = 0; k < 4; ++k) sm += (blo(xa[k]) + bhi(xa[k])) + (blo(xb[k]) + bhi(xb[k])) + (v3 ? blo(xc[k]) + bhi(xc[k]) : 0.f);
    const float mean = rsum32(sm) * (1.f / NM);
    float sq = 0.f;
#pragma unroll
    for (int k = 0; k < 4; ++k) {
      float d;
      d = blo(xa[k]) - mean; sq += d * d; d = bhi(xa[k]) - mean; sq += d * d; d = blo(xb[k]) - mean; sq += d * d; d = bhi(xb[k]) - mean; sq += d * d;
      d = blo(xc[k]) - mean; sq += v3 ? d * d : 0.f; d = bhi(xc[k]) - mean; sq += v3 ? d * d : 0.f;
    }
    const float var = rsum32(sq) * (1.f / NM);
    const float mb = rbf(mean), sd = rbf(sqrtf(rbf(rbf(var) + 1e-5f)));
    const float mb0 = rdl(mb, 0), mb1 = rdl(mb, 32), sd0 = rdl(sd, 0), sd1 = rdl(sd, 32);
    const float m_ = (t & 1) ? mb1 : mb0, s_ = (t & 1) ? sd1 : sd0;
    ln0 = rbf(rbf(blo(xm) - m_) / s_); ln1 = rbf(rbf(bhi(xm) - m_) / s_);
    PIN(ln0); PIN(ln1);
    if ((t & 223) == 0) LDS_WRF(L_MU + (t >> 5) * 4, mean);
  }
  if (w >= 3) VMW(0);
  BAR();
  const int wh = t & 1, o1 = WS_H1 + (wh * NM + 160 * j + min(t >> 1, 79) * 2) * 8;
  v4u v[5];
  if (w < 3) {
#pragma unroll
    for (int k = 0; k < 5; ++k) v[k] = ld16(ws, o1 + k * 10240);
  }
  if (w == 3) {
    const unsigned f = poll_votes(ws, tag, lane);
    if (lane == 0) LDS_WR32(L_FLIP, f);
  }
  SCHED();
  v4u wf[5];
  {
    const int a = L_WQ + tr_lane<256>(w, lane);
#pragma unroll
    for (int ks = 0; ks < 5; ++ks) wf[ks] = trfrag<256>(lds, a + ks * 8192);
  }
#pragma unroll
  for (int ks = 0; ks < 5; ++ks) PIN(wf[ks]);
  const int h2b = WS_H2 + (h & 15) * 16384;
  int hsr = L_HS + (i & 1) * 320 + g4 * 16, h2o = h2b + (j * 8 + w) * 256 + (i * 4 + g4) * 16, h2i = h2b + (g4 * 8 + w) * 256 + i * 16;
  int qhw = L_QH + ((lane >> 2) & 1) * 256 + (16 * w + 64 * (lane >> 5) + 4 * (lane & 3) + 2 * ((lane >> 3) & 1) + (g4 & 1)) * 2, qhr = L_QH + (i & 1) * 256 + g4 * 16;
  int h3o = WS_H3 + (h >> 3) * 16384 + (i & 1) * 8192 + (h & 7) * 1024 + (128 * j + 32 * w + 8 * g4) * 2;
  PIN(hsr); PIN(h2o); PIN(h2i); PIN(qhw); PIN(qhr); PIN(h3o);
  if (w < 3) {
    int hsw = L_HS + wh * 320 + min(t >> 1, 79) * 4; PIN(hsw);
    const unsigned shk = wh ? 0u : 16u, shs = wh ? 16u : 0u;
    float k0, k1, s0, s1;
#pragma unroll 1
    for (unsigned sp = 0; sp < SPIN_CAP; ++sp) {
      unsigned bad = 0;
      k0 = k1 = s0 = s1 = 0.f;
#pragma unroll
      for (int k = 0; k < 5; ++k) {
        bad |= (v[k].y ^ tag) | (v[k].w ^ tag);
        s0 += bhi(v[k].x << shs); s1 += bhi(v[k].z << shs); k0 += bhi(v[k].x << shk); k1 += bhi(v[k].z << shk);
      }
      PIN(k0); PIN(k1); PIN(s0); PIN(s1);
      if (__all(bad == 0u)) break;
      RELOAD();
#pragma unroll
      for (int k = 0; k < 5; ++k) v[k] = ld16(ws, o1 + k * 10240);
    }
    const float rc0 = dpp<0xB1>(s0), rc1 = dpp<0xB1>(s1);
    const float sa0 = wh ? rc0 : k0, sa1 = wh ? rc1 : k1, ca0 = wh ? k0 : rc0, ca1 = wh ? k1 : rc1;
    LDS_WR32(hsw, pk(fmaf(ln0, ca0, ln0 + sa0), fmaf(ln1, ca1, ln1 + sa1)));
  }
  BAR();
  int fl, myc;
  {
    v4u hf[5];
#pragma unroll
    for (int ks = 0; ks < 5; ++ks) hf[ks] = LDS_RD128(hsr + ks * 64);
    const unsigned flv = *(const LAS unsigned*)(lds + L_FLIP);
    SCHED();
    f4 d = {0.f, 0.f, 0.f, 0.f}, d2 = d;
    d = mfma(wf[0], hf[0], d); d2 = mfma(wf[1], hf[1], d2); d = mfma(wf[2], hf[2], d); d2 = mfma(wf[3], hf[3], d2); d = mfma(wf[4], hf[4], d);
    SCHED();
    fl = __builtin_amdgcn_readfirstlane(flv); myc = (iod ^ fl) * 8192;
    d += d2;
    if (i < 2) {
      st16s(ws, h2o, myc, (v4u){__float_as_uint(d[0]), tag, __float_as_uint(d[1]), tag});
      st16s(ws, h2o + 128, myc, (v4u){__float_as_uint(d[2]), tag, __float_as_uint(d[3]), tag});
    }
  }
  int uka = L_UK + (32 * (w & 3) + 8 * (i >> 2) + (i & 3)) * 256 + ((g4 ^ i) << 4);
  PIN(uka);
  h3o ^= fl << 13;
  if (w >= 4) {
    const rsrc_t ru = mkrs(p.wuk, DC * HD * 2);
    const int so = (128 * j * HD + h * 128) * 2 + (w - 4) * 32 * HD * 2, ul = g4 * HD * 2 + ((i ^ g4) << 4);
#pragma unroll
    for (int n = 0; n < 8; ++n) dma(ru, ul ^ ((n >> 1) << 6), so + n * 4 * HD * 2, L_UK + (w - 4) * 8192 + n * 1024);
    VMW(0);
  } else {
    v4u v, v2;
    for (unsigned sp = 0; sp < SPIN_CAP; ++sp) { RELOAD(); v = ld16s(ws, h2i, myc); v2 = ld16s(ws, h2i + 1024, myc); if (__all((v.y == tag) & (v.w == tag) & (v2.y == tag) & (v2.w == tag))) break; }
    const float sc = 0.08838834764831845f * 1.4426950408889634f;
    const float q0 = sum16(__uint_as_float(v.x), __uint_as_float(v.z)), q1 = sum16(__uint_as_float(v2.x), __uint_as_float(v2.z));
    *(LAS u16*)(lds + qhw) = (u16)pk(sum32(q0, q1) * sc, 0.f);
  }
  BAR();
  if (w < 4) {
    v4u qf[4], uk[8];
#pragma unroll
    for (int ks = 0; ks < 4; ++ks) { qf[ks] = LDS_RD128(qhr + ks * 64); uk[ks] = LDS_RD128(uka ^ (ks << 6)); uk[4 + ks] = LDS_RD128((uka ^ (ks << 6)) + 1024); }
    SCHED();
    f4 d0 = {0.f, 0.f, 0.f, 0.f}, d1 = d0;
#pragma unroll
    for (int ks = 0; ks < 4; ++ks) { d0 = mfma(uk[ks], qf[ks], d0); d1 = mfma(uk[4 + ks], qf[ks], d1); }
    if (i < 2) st16(ws, h3o, (v4u){nosent(pk(d0[0], d0[1])), nosent(pk(d0[2], d0[3])), nosent(pk(d1[0], d1[1])), nosent(pk(d1[2], d1[3]))});
  }
  BAR();
  {
    const rsrc_t rv = mkrs(p.wuv, DC * HD * 2);
    const int so = (128 * j * HD + h * 128) * 2 + w * 16 * HD * 2;
#pragma unroll
    for (int n = 0; n < 4; ++n) dma(rv, (n & 2) ? dl1 : dl0, so + n * 4 * HD * 2, L_WUV + w * 4096 + n * 1024);
  }
  if (r6) {
    const rsrc_t r1 = mkrs(p.w1, NM * FF * 2);
    const int w1l = dma_lane<64>(FF * 2, lane, 0);
#pragma unroll 1
    for (int n = 0; n < 5; ++n) { int in = n * 8 + w + (t6 & 1) * 20; if (in >= 40) in -= 40; dma(r1, w1l, 64 * t6 + in * 16 * FF * 2, L_ROLE + in * 1024); }
    VMW(5);
  } else if (rc >= 8) {
    const int u = ((rc - 8) * 8 + w) * 50 + lane;
    l2pf(mkrs(p.wmod, NM * 3840 * 2), lane < 50 ? (u / 5) * 7680 + 2560 + xcd * 640 + (u % 5) * 128 : 0x7ffffff0, L_SCR);
    VMW(1);
  } else { VMW(0); }
  BAR();
  v4u uv[16];
#pragma unroll
  for (int nt = 0; nt < 8; ++nt) {
    const int a = L_WUV + (w & 1) * 16384 + tr_lane<256>(nt, lane);
    uv[nt] = trfrag<256>(lds, a); uv[8 + nt] = trfrag<256>(lds, a + 8192);
  }
#pragma unroll
  for (int n = 0; n < 16; ++n) PIN(uv[n]);
  v4u w1f[5];
  const int tq = min(t, 319), bb6 = tq >= 160, mq = tq - 160 * bb6;
  v2u xx = {0u, 0u}; unsigned b1v = 0;
  if (r6) {
    xx = *(const GAS v2u*)(p.x + bb6 * NM + 4 * mq);
    b1v = *(const GAS unsigned*)(p.b1 + 32 * t6 + 2 * (t & 15));
    VMW(0);
    BAR();
    const int a = L_ROLE + 160 * (w >> 1) * 64 + tr_lane<64>(w & 1, lane);
#pragma unroll
    for (int ks = 0; ks < 5; ++ks) w1f[ks] = trfrag<64>(lds, a + ks * 2048);
#pragma unroll
    for (int ks = 0; ks < 5; ++ks) PIN(w1f[ks]);
    PIN(xx); PIN(b1v);
  }
  {
    const int bb = w >> 2, sh = (w >> 1) & 1, kh = w & 1;
    const int os = WS_ST + myc + (bb * 32 + 16 * (sh ^ ((i >> 2) & 1)) + 4 * g4 + (i & 3)) * 128 + (h & 15) * 8;
    const int oa = WS_H4 + (((h & 15) * 4 + j) * 2 + bb) * 16384 + (myc ^ 8192) + (16 * sh + i) * 256 + kh * 128 + g4 * 16;
    int rw = L_CRED + (w * 128 + 32 * g4 + i) * 4, rd = L_CRED + ((t >> 5) * 512 + 4 * (t & 31)) * 4;
    int o5 = WS_H5 + j * 16384 + myc + ((t >> 5) & 1) * 4096 + (h & 15) * 256 + (t & 31) * 8;
    PIN(rw); PIN(rd); PIN(o5);
    v2u sv = {0u, 0u};
    v4u a0, a1;
    int oa0 = oa, oa1 = oa + 64; PIN(oa0); PIN(oa1);
    for (unsigned sp = 0; sp < SPIN_CAP; ++sp) { RELOAD(); if (i < 8) sv = ld8(ws, os); if (__all(max(sv.x, sv.y) != SENT)) break; }
    a0 = ld16(ws, oa0); a1 = ld16(ws, oa1);
    SCHED();
    const float mv = i < 8 ? __uint_as_float(sv.x) : -__builtin_inff(), lv = i < 8 ? __uint_as_float(sv.y) : 0.f;
    float M = vmax(mv, dpp<0xB1>(mv)); M = vmax(M, dpp<0x4E>(M)); M = vmax(M, dpp<0x141>(M)); M = vmax(M, dpp<0x140>(M)); M = max32(max16(M));
    const float e = __builtin_amdgcn_exp2f(mv - M);
    float L = e * lv; L += dpp<0xB1>(L); L += dpp<0x4E>(L); L += dpp<0x141>(L); L += dpp<0x140>(L); L = sum16(L, L); L = sum32(L, L);
    const float wg = e * __builtin_amdgcn_rcpf(L);
    f2 w01 = {dpp<0x150>(wg), dpp<0x151>(wg)}, w23 = {dpp<0x152>(wg), dpp<0x153>(wg)};
    PIN(w01); PIN(w23);
#pragma unroll 1
    for (unsigned sp = 0; sp < SPIN_CAP; ++sp) { if (__all(max(mx4(a0), mx4(a1)) != SENT)) break; RELOAD(); a0 = ld16(ws, oa0); a1 = ld16(ws, oa1); }
    f4 d[8];
    float o[8];
#pragma unroll
    for (int q = 0; q < 4; ++q) {
      d[2 * q] = mfma(a0, uv[2 * q], (f4){0.f, 0.f, 0.f, 0.f}); d[2 * q + 1] = mfma(a0, uv[2 * q + 1], (f4){0.f, 0.f, 0.f, 0.f});
      d[2 * q] = mfma(a1, uv[8 + 2 * q], d[2 * q]); d[2 * q + 1] = mfma(a1, uv[9 + 2 * q], d[2 * q + 1]);
    }
#pragma unroll
    for (int nt = 0; nt < 8; ++nt) { const f2 y = w01 * (f2){d[nt][0], d[nt][1]} + w23 * (f2){d[nt][2], d[nt][3]}; o[nt] = y[0] + y[1]; }
    const float u0 = sum32(o[0], o[4]), u1 = sum32(o[1], o[5]), u2 = sum32(o[2], o[6]), u3 = sum32(o[3], o[7]);
    const float v0 = sum16(u0, u2), v1 = sum16(u1, u3);
    LDS_WRF(rw, v0); LDS_WRF(rw + 64, v1);
    BAR();
    if (w == 0) {
      const f4 a = (__builtin_bit_cast(f4, LDS_RD128(rd)) + __builtin_bit_cast(f4, LDS_RD128(rd + 512))) + (__builtin_bit_cast(f4, LDS_RD128(rd + 1024)) + __builtin_bit_cast(f4, LDS_RD128(rd + 1536)));
      st8(ws, o5, (v2u){nosent(pk(a[0], a[1])), nosent(pk(a[2], a[3]))});
    }
  }
  if (rc >= 8) {
    const int row = ((rc - 8) * 8 + w) * 27 + (lane >> 1);
    if (w != 0) __builtin_amdgcn_s_sleep(6);
    l2pf(mkrs(p.w2, FF * NM * 2), (lane < 54) ? row * NM * 2 + ((160 * xcd) & ~127) + (lane & 1) * 128 : 0x7ffffff0, L_SCR);
  }
  if (r6) {
    const int bb = bb6;
    float xv[4], gav[4], cbv[4];
    float b1a = blo(b1v), b1b = bhi(b1v);
    if (w < 5) {
      v4u ga, sb, cbb;
      const int om = WS_MV + myc + mq * 16;
      for (unsigned sp = 0; sp < SPIN_CAP; ++sp) { RELOAD(); ga = ld16(ws, om); sb = ld16(ws, om + 2560); cbb = ld16(ws, om + 5120); if (__all(nos4(ga) & nos4(sb) & nos4(cbb))) break; }
      const float mu0 = LDS_RDF(L_MU + bb * 4);
      xv[0] = blo(xx.x) - mu0; xv[1] = bhi(xx.x) - mu0; xv[2] = blo(xx.y) - mu0; xv[3] = bhi(xx.y) - mu0;
      float sbv[4];
#pragma unroll
      for (int k = 0; k < 4; ++k) { gav[k] = bb ? bhi(ga[k]) : blo(ga[k]); sbv[k] = bb ? bhi(sb[k]) : blo(sb[k]); cbv[k] = rbf(1.f + (bb ? bhi(cbb[k]) : blo(cbb[k]))); }
      LDS_WR64(L_GS + bb * 1280 + mq * 8, ((v2u){pk(cbv[0], cbv[1]), pk(cbv[2], cbv[3])}));
      LDS_WR64(L_GS + 2560 + bb * 1280 + mq * 8, ((v2u){pk(sbv[0], sbv[1]), pk(sbv[2], sbv[3])}));
    }
    BAR();
    {
      f4 d = {0.f, 0.f, 0.f, 0.f};
      v4u gf[5];
#pragma unroll
      for (int ks = 0; ks < 5; ++ks) gf[ks] = LDS_RD128(L_GS + (i & 3) * 1280 + 320 * (w >> 1) + g4 * 16 + ks * 64);
      SCHED();
#pragma unroll
      for (int ks = 0; ks < 5; ++ks) d = mfma(gf[ks], w1f[ks], d);
      if (lane < 16) {
#pragma unroll
        for (int q = 0; q < 4; ++q) LDS_WRF(L_GS + 5120 + (((w >> 1) * 4 + q) * 32 + (w & 1) * 16 + lane) * 4, d[q]);
      }
    }
    const int o6 = WS_H6 + myc + bb * 1280 + mq * 8;
    int scw = L_SC + bb * 1280 + mq * 8, scr = L_SC + (i & 1) * 1280 + 320 * (w >> 1) + g4 * 16, str = L_CRED + ((t >> 4) & 1) * 64;
    int stw = L_CRED + bb * 64 + ((t >> 5) - 5 * bb) * 8, rw = L_CRED + 128 + ((w >> 1) * 64 + (w & 1) * 16 + i) * 4, rd = L_CRED + 128 + 2 * (t & 31) * 4;
    int o7 = WS_H7 + 128 * t6 + (t & 28) * 4 + (t & 1) * 8192;
    PIN(scw); PIN(scr); PIN(str); PIN(o7); PIN(stw); PIN(rw); PIN(rd); PIN(b1a); PIN(b1b);
    int gsr = L_GS + 5120 + (((t >> 4) & 1) * 32 + 2 * (t & 15)) * 4; PIN(gsr);
#pragma unroll
    for (int k = 0; k < 4; ++k) { PIN(gav[k]); PIN(cbv[k]); PIN(xv[k]); }
#pragma unroll
    for (int ks = 0; ks < 5; ++ks) PIN(w1f[ks]);
    float z[4] = {0.f, 0.f, 0.f, 0.f};
    if (w < 5) {
      float a[4];
      for (unsigned sp = 0; sp < SPIN_CAP; ++sp) {
        RELOAD();
        v2u pa[8];
        unsigned mm = 0;
#pragma unroll
        for (int k = 0; k < 8; ++k) pa[k] = ld8(ws, o6 + (k >> 1) * 16384 + (k & 1) * 4096);
        a[0] = a[1] = a[2] = a[3] = 0.f;
#pragma unroll
        for (int k = 0; k < 8; ++k) { mm = max(mm, max(pa[k].x, pa[k].y)); a[0] += blo(pa[k].x); a[1] += bhi(pa[k].x); a[2] += blo(pa[k].y); a[3] += bhi(pa[k].y); }
        PIN(a[0]); PIN(a[1]); PIN(a[2]); PIN(a[3]);
        if (__all(mm != SENT)) break;
      }
#pragma unroll
      for (int k = 0; k < 4; ++k) z[k] = fmaf(gav[k], a[k], xv[k]);
      LDS_WR64(scw, ((v2u){pk(z[0] * cbv[0], z[1] * cbv[1]), pk(z[2] * cbv[2], z[3] * cbv[3])}));
    }
    BAR();
    v4u hf[5];
#pragma unroll
    for (int ks = 0; ks < 5; ++ks) hf[ks] = LDS_RD128(scr + ks * 64);
    v2u gv[4], sv[4];
#pragma unroll
    for (int k = 0; k < 4; ++k) { gv[k] = LDS_RD64(gsr + k * 512); sv[k] = LDS_RD64(gsr + k * 512 + 256); }
    SCHED();
    const float s1 = rsum32((z[0] + z[1]) + (z[2] + z[3])), s2 = rsum32((z[0] * z[0] + z[1] * z[1]) + (z[2] * z[2] + z[3] * z[3]));
    f4 d = {0.f, 0.f, 0.f, 0.f}, d2 = d;
    d = mfma(hf[0], w1f[0], d); d2 = mfma(hf[1], w1f[1], d2); d = mfma(hf[2], w1f[2], d); d2 = mfma(hf[3], w1f[3], d2); d = mfma(hf[4], w1f[4], d);
    const float gs0 = (__uint_as_float(gv[0].x) + __uint_as_float(gv[1].x)) + (__uint_as_float(gv[2].x) + __uint_as_float(gv[3].x));
    const float gs1 = (__uint_as_float(gv[0].y) + __uint_as_float(gv[1].y)) + (__uint_as_float(gv[2].y) + __uint_as_float(gv[3].y));
    b1a += (__uint_as_float(sv[0].x) + __uint_as_float(sv[1].x)) + (__uint_as_float(sv[2].x) + __uint_as_float(sv[3].x));
    b1b += (__uint_as_float(sv[0].y) + __uint_as_float(sv[1].y)) + (__uint_as_float(sv[2].y) + __uint_as_float(sv[3].y));
    if (w < 5 && (lane & 31) == 0) LDS_WR64(stw, ((v2u){__float_as_uint(s1), __float_as_uint(s2)}));
    if (lane < 16) { LDS_WRF(rw, d[0] + d2[0]); LDS_WRF(rw + 128, d[1] + d2[1]); }
    BAR();
    if (w == 0) {
      v2u v[4];
      const f4 sa = __builtin_bit_cast(f4, LDS_RD128(str)), sbq = __builtin_bit_cast(f4, LDS_RD128(str + 16));
      const v2u sc_ = LDS_RD64(str + 32);
#pragma unroll
      for (int k = 0; k < 4; ++k) v[k] = LDS_RD64(rd + k * 256);
      SCHED();
      const float dl = (((sa[0] + sa[2]) + (sbq[0] + sbq[2])) + __uint_as_float(sc_.x)) * (1.f / NM);
      const float q2 = (((sa[1] + sa[3]) + (sbq[1] + sbq[3])) + __uint_as_float(sc_.y)) * (1.f / NM);
      const float rs = __builtin_amdgcn_rsqf(fmaxf(fmaf(-dl, dl, q2), 0.f) + 1e-5f);
      const float a0 = (__uint_as_float(v[0].x) + __uint_as_float(v[1].x)) + (__uint_as_float(v[2].x) + __uint_as_float(v[3].x));
      const float a1 = (__uint_as_float(v[0].y) + __uint_as_float(v[1].y)) + (__uint_as_float(v[2].y) + __uint_as_float(v[3].y));
      const float r0 = fmaxf(fmaf(rs, fmaf(-dl, gs0, a0), b1a), 0.f), r1 = fmaxf(fmaf(rs, fmaf(-dl, gs1, a1), b1b), 0.f);
      const v4u o = quad_gather(nosent(pk(r0 * r0, r1 * r1)));
      if (t < 32 && (t & 3) < 2) st16(ws, o7, o);
    }
  }
}

__global__ void __launch_bounds__(512, 2) fused_decode(Params p) {
  extern __shared__ __attribute__((aligned(16))) unsigned char lds_raw[];
  LAS unsigned char* lds = (LAS unsigned char*)(unsigned)0;
  asm volatile("" :: "s"((unsigned)(size_t)(LAS unsigned char*)lds_raw));
  const int g = blockIdx.x, w = __builtin_amdgcn_readfirstlane(threadIdx.x >> 6), lane = threadIdx.x & 63;
  if (w == 7) {
    unsigned long long pc;
    asm volatile("s_getpc_b64 %0" : "=s"(pc));
    const rsrc_t rc = __builtin_amdgcn_make_buffer_rsrc((void*)pc, 0, CODE_LEN, 0x00020000);
    const int vo = ((g >> 3) + 32 * (lane & 15)) * 128;
    asm volatile("s_mov_b32 m0, %0\n\ts_nop 0\n\tbuffer_load_dword %1, %2, 0 offen lds" :: "s"(L_SCR), "v"(lane < 16 ? vo : 0x7ffffff0), "s"(rc) : "m0", "memory");
  }
  asm volatile("" :: "s"(p.x), "s"(p.ckv), "s"(p.wq), "s"(p.wuk), "s"(p.wuv), "s"(p.wo), "s"(p.w1), "s"(p.b1), "s"(p.c), "s"(p.wmod), "s"(p.ws));
  const rsrc_t ws = __builtin_amdgcn_make_buffer_rsrc(p.ws, 0, WS_BYTES, 0x00020000);
  const bool s0 = g < 100;
  const int cb = g % 20, ks0 = g / 20, t = threadIdx.x;
  unsigned tag0 = 0;
  if (s0) {
    const int so = (128 * ks0 * 3840 + 64 * cb) * 2 + w * 16 * 7680;
    const rsrc_t rm = mkrs(p.wmod, NM * 3840 * 2);
    if (w < 4) {
      unsigned e0, cv;
      int s0a = L_QS + tr_lane<128>(w, lane), s0s = L_PB + (lane & 1) * 256 + (lane >> 4) * 16, h1o = WS_H1 + ks0 * 10240 + (64 * cb + 16 * w + lane) * 8;
      asm volatile("buffer_load_ushort %1, %3, %4, %5 offen\n\tbuffer_load_dword %0, off, %2, 0\n\t"
                   "s_mov_b32 m0, %6\n\ts_nop 0\n\tbuffer_load_dwordx4 %7, %8, %9 offen lds\n\t"
                   "s_mov_b32 m0, %10\n\ts_nop 0\n\tbuffer_load_dwordx4 %11, %8, %12 offen lds"
                   : "=&v"(e0), "=&v"(cv)
                   : "s"(ws), "v"(((t >> 7) * NM + (t & 127)) * 2), "s"(mkrs(p.c, 2560)), "s"(256 * ks0),
                     "s"(L_QS + w * 2048), "v"(dma_lane<128>(7680, lane, 0)), "s"(rm), "s"(so),
                     "s"(L_QS + w * 2048 + 1024), "v"(dma_lane<128>(7680, lane, 1)), "s"(so + 8 * 7680)
                   : "m0", "memory");
      asm volatile("s_waitcnt vmcnt(2)" : "+v"(e0), "+v"(cv), "+v"(s0a), "+v"(s0s), "+v"(h1o) :: "memory");
      tag0 = mktag(e0);
      const float a = blo(cv);
      *(LAS u16*)(lds + L_PB + t * 2) = (u16)pk(a * __builtin_amdgcn_rcpf(1.f + __expf(-a)), 0.f);
      VMW(0);
      BAR();
      f4 acc = {0.f, 0.f, 0.f, 0.f}, acc2 = acc;
      v4u bf[4], sa[4];
#pragma unroll
      for (int ks = 0; ks < 4; ++ks) { bf[ks] = trfrag<128>(lds, s0a + ks * 4096); sa[ks] = LDS_RD128(s0s + ks * 64); }
      SCHED();
      acc = mfma(sa[0], bf[0], acc); acc2 = mfma(sa[1], bf[1], acc2); acc = mfma(sa[2], bf[2], acc); acc2 = mfma(sa[3], bf[3], acc2);
      if (lane < 16) st8(ws, h1o, (v2u){pk(acc[0] + acc2[0], acc[1] + acc2[1]), tag0});
    } else {
      dma(rm, dma_lane<128>(7680, lane, 0), so, L_QS + w * 2048);
      dma(rm, dma_lane<128>(7680, lane, 1), so + 8 * 7680, L_QS + w * 2048 + 1024);
      VMW(0);
      BAR();
    }
  } else if (g < 128) {
    if (w == 0) tag0 = ldtag(ws);
    if (w == 1 || w == 3) {
      const v4u sv = {SENT, SENT, SENT, SENT};
#pragma unroll 1
      for (int ch = (g - 100) * 2 + (w >> 1); ch < 512; ch += 56) st16(ws, WS_CLR + ch * 1024 + lane * 16, sv);
      VMW(0);
    } else if (w == 2) {
      unsigned tg;
      const int pa = WS_PR + ((g - 100) % 6) * 16384 + (((g - 100) / 6) & 1) * 4096 + (g - 100) * 128;
      unsigned long long t0, t1, t2, t3;
      if (lane == 0) { st4(ws, pa, 0u); st4(ws, pa + 8192, 0u); }
      asm volatile("s_waitcnt vmcnt(1)\n\ts_memtime %0\n\ts_waitcnt vmcnt(0)\n\ts_memtime %1\n\ts_waitcnt lgkmcnt(0)" : "=&s"(t0), "=&s"(t1) :: "memory");
      if (lane == 0) { st4(ws, pa + 8192, 0u); st4(ws, pa, 0u); }
      asm volatile("buffer_load_dword %2, off, %3, 0\n\ts_waitcnt vmcnt(2)\n\ts_memtime %0\n\ts_waitcnt vmcnt(1)\n\ts_memtime %1\n\ts_waitcnt vmcnt(0) lgkmcnt(0)" : "=&s"(t2), "=&s"(t3), "=&v"(tg) : "s"(ws) : "memory");
      const unsigned vote = (((unsigned)(t1 - t0) > (unsigned)(t3 - t2)) ? 0u : 1u) ^ ((g >> 2) & 1);
      if (lane < 2) st8(ws, WS_CF + lane * 8192 + (g - 100) * 128, (v2u){mktag(tg), vote});
    }
    BAR();
    if (w == 0 && lane < 2) st8(ws, WS_CF + lane * 8192 + (g - 100) * 128 + 8, (v2u){tag0, 0u});
  }
  if ((g >> 3) < 16) a_path(p, lds, ws, g, w, lane, tag0); else c_path(p, lds, ws, g, w, lane);
}

extern "C" void kernel_launch(void* const* d_in, const int* in_sizes, int n_in,
                              void* d_out, int out_size, void* d_ws, size_t ws_size,
                              hipStream_t stream) {
  Params p;
  p.x = (const u16*)d_in[0]; p.ckv = (const u16*)d_in[1]; p.wq = (const u16*)d_in[2]; p.wuk = (const u16*)d_in[3];
  p.wuv = (const u16*)d_in[4]; p.wo = (const u16*)d_in[5]; p.w1 = (const u16*)d_in[7]; p.b1 = (const u16*)d_in[8];
  p.w2 = (const u16*)d_in[9]; p.b2 = (const u16*)d_in[10]; p.c = (const u16*)d_in[11]; p.wmod = (const u16*)d_in[12];
  p.out = (u16*)d_out; p.ws = (unsigned char*)d_ws + WS_OFF;
  (void)hipFuncSetAttribute((const void*)fused_decode, hipFuncAttributeMaxDynamicSharedMemorySize, LDS_BYTES);
  hipLaunchKernelGGL(fused_decode, dim3(256), dim3(512), LDS_BYTES, stream, p);
}
```
